```python
import torch

def _softmax(x, axis=-1):
    return torch.softmax(x, dim=axis)

def _gelu(x):
    return torch.nn.functional.gelu(x, approximate='tanh')

def _silu(x):
    return torch.nn.functional.silu(x)

def _sigmoid(x):
    return torch.sigmoid(x)

def _layernorm(x, axis=-1, eps=1e-05):
    m = x.mean(dim=axis, keepdim=True)
    v = x.var(dim=axis, keepdim=True, unbiased=False)
    return (x - m) / torch.sqrt(v + eps)

def _scatter_add(tgt, idx, src, axis=0):
    return tgt.clone().index_add_(axis, idx, src)

def _segment_sum(src, seg, n):
    out = torch.zeros((n,) + src.shape[1:], dtype=src.dtype)
    return out.index_add_(0, seg, src)

def _pad(x, pw):
    flat = tuple((v for pair in reversed(pw) for v in pair))
    return torch.nn.functional.pad(x, flat)
kernel_name = 'gated_mix__memory_aug_recurrent_mem_tr__mixer_channel_mlp__prompt'

def setup_inputs():

    _w = lambda a: (lambda t: t.to(torch.bfloat16) if t.is_floating_point() else t)(torch.as_tensor(a))

    def _A_setup():
        B, S, Dm = (1, 2048, 448)
        H, Dh = (16, 256)
        M, seg = (8, 32)
        seg = min(seg, S)
        S = S // seg * seg
        return {'x': _w((lambda *s: torch.randn(*s, dtype=torch.bfloat16) * 0.05)(B, S, Dm)), 'mem0': _w((lambda *s: torch.randn(*s, dtype=torch.bfloat16) * 0.8)(B, M, Dm)), 'Wq': _w((lambda *s: torch.randn(*s, dtype=torch.bfloat16) * 1.6)(Dm, H, Dh) * Dm ** (-0.5)), 'Wk': _w((lambda *s: torch.randn(*s, dtype=torch.bfloat16) * 1.6)(Dm, H, Dh) * Dm ** (-0.5)), 'Wv': _w((lambda *s: torch.randn(*s, dtype=torch.bfloat16) * 0.8)(Dm, H, Dh) * Dm ** (-0.5)), 'Wo': _w((lambda *s: torch.randn(*s, dtype=torch.bfloat16) * 0.8)(H, Dh, Dm) * (H * Dh) ** (-0.5)), 'seg_len': seg}
    _A_in = _A_setup()

    def _B_setup():
        B, S, D, F = (1, 2048, 448, 1792)
        return {'x': _w((lambda *s: torch.randn(*s, dtype=torch.bfloat16) * 0.05)(B, S, D)), 'w1': _w((lambda *s: torch.randn(*s, dtype=torch.bfloat16) * 1.6)(D, F) * D ** (-0.5)), 'b1': _w((lambda *s: torch.randn(*s, dtype=torch.bfloat16) * 0.4)(F)), 'w2': _w((lambda *s: torch.randn(*s, dtype=torch.bfloat16) * 0.05)(F, D) * F ** (-0.5)), 'b2': _w((lambda *s: torch.randn(*s, dtype=torch.bfloat16) * 0.05)(D))}
    _B_in = _B_setup()
    return {'A_x': _A_in['x'], 'A_mem0': _A_in['mem0'], 'A_Wq': _A_in['Wq'], 'A_Wk': _A_in['Wk'], 'A_Wv': _A_in['Wv'], 'A_Wo': _A_in['Wo'], 'A_seg_len': _A_in['seg_len'], 'B_x': _B_in['x'], 'B_w1': _B_in['w1'], 'B_b1': _B_in['b1'], 'B_w2': _B_in['w2'], 'B_b2': _B_in['b2'], 'gate_w': _w((lambda *s: torch.randn(*s, dtype=torch.bfloat16) * 0.05)(448) * 16.0)}

def reference(A_x, A_mem0, A_Wq, A_Wk, A_Wv, A_Wo, A_seg_len, B_x, B_w1, B_b1, B_w2, B_b2, gate_w):

    def _A_ref(x, mem0, Wq, Wk, Wv, Wo, seg_len):
        B, S, Dm = x.shape
        H, Dh = (Wq.shape[1], Wq.shape[2])
        M = mem0.shape[1]
        scale = Dh ** (-0.5)
        n_seg = S // seg_len
        L = M + seg_len
        idx = torch.arange(L)
        mask = (idx[:, None] < M) | (idx[:, None] >= idx[None, :])
        mem = mem0
        outs = []
        for s in range(n_seg):
            xs = x[:, s * seg_len:(s + 1) * seg_len, :].clone()
            h = torch.concatenate([mem, xs], axis=1)
            q = torch.einsum('btd,dhe->bhte', h, Wq)
            k = torch.einsum('btd,dhe->bhte', h, Wk)
            v = torch.einsum('btd,dhe->bhte', h, Wv)
            scores = torch.einsum('bhqe,bhke->bhqk', q, k) * scale
            scores = torch.where(mask[None, None], scores, -1000000000.0)
            p = _softmax(scores, axis=-1)
            o = torch.einsum('bhqk,bhke->bhqe', p, v)
            o = torch.einsum('bhte,hed->btd', o, Wo)
            mem = o[:, :M, :].clone()
            outs.append(o[:, M:, :])
        return torch.concatenate(outs, axis=1)

    def _B_ref(x, w1, b1, w2, b2):
        y = _layernorm(x, axis=-1)
        h = _gelu(torch.einsum('bsd,df->bsf', y, w1) + b1)
        return torch.einsum('bsf,fd->bsd', h, w2) + b2
    x = A_x
    g = _sigmoid((x * gate_w).sum(-1, keepdims=True))
    a = _A_ref(x, A_mem0, A_Wq, A_Wk, A_Wv, A_Wo, A_seg_len)
    b = _B_ref(x, B_w1, B_b1, B_w2, B_b2)
    return g * a + (1 - g) * b
```

```cpp
#include <hip/hip_runtime.h>
#include <hip/hip_bf16.h>

#define DEV __device__ __forceinline__
#define LAS __attribute__((address_space(3)))
typedef __bf16 bf2 __attribute__((ext_vector_type(2)));
typedef __bf16 bf8 __attribute__((ext_vector_type(8)));
typedef __bf16 bf16v __attribute__((ext_vector_type(16)));
typedef float f2 __attribute__((ext_vector_type(2)));
typedef float f4 __attribute__((ext_vector_type(4)));
typedef unsigned u2 __attribute__((ext_vector_type(2)));
typedef unsigned u4 __attribute__((ext_vector_type(4)));
typedef unsigned u8v __attribute__((ext_vector_type(8)));
typedef short s4 __attribute__((ext_vector_type(4)));
typedef __amdgpu_buffer_rsrc_t rsrc_t;
typedef LAS char* lptr;

constexpr unsigned SET = 28u * 14u * 1024u;
constexpr unsigned O_AW = 0;
constexpr unsigned O_CW = O_AW + 16 * SET;
constexpr unsigned O_W1 = O_CW + 16 * SET;
constexpr unsigned O_W2 = O_W1 + 4 * SET;
constexpr unsigned O_KM = O_W2 + 4 * SET;
constexpr unsigned O_XA = O_KM + 64u * 16u * 7168u;
constexpr unsigned O_YA = O_XA + 128u * 14336u;
constexpr unsigned O_UB = O_YA + 128u * 14336u;
constexpr unsigned O_QB = O_UB + 64u * 448u * 1024u;
constexpr unsigned BMSZ = 2048u * 448u * 4u;
constexpr unsigned O_BM = O_QB + 64u * 448u * 1024u;
constexpr unsigned O_GT = O_BM + 4 * BMSZ;
constexpr unsigned O_WB = O_GT + 8192;
constexpr unsigned O_PX = O_WB + 64u * 14u * 8192u;
constexpr unsigned O_PM = O_PX + 64u * 16u * 2048u;
constexpr unsigned MSTR = 2 * 7168, PSTR = 9216 + 2 * 8192;
constexpr unsigned O_MEM = O_PM + 64u * 8192u;
constexpr unsigned O_PB = O_MEM + 65u * MSTR;
constexpr unsigned O_FL = O_PB + 64u * PSTR;
constexpr unsigned F_U = O_FL;
constexpr unsigned F_Q = F_U + 32 * 256;
constexpr unsigned F_K = F_Q + 32 * 256;
constexpr unsigned F_B = F_K + 64 * 256;
constexpr unsigned F_P = F_B + 16 * 256;
constexpr unsigned F_S = F_P + 64 * 256;
constexpr unsigned F_AB = F_S + 256;
constexpr unsigned O_END = F_AB + 256;
constexpr unsigned WS_BYTES = O_END;
#define SPINLOG 21
constexpr unsigned SPIN_CAP = 1u << SPINLOG;
constexpr int LDS_MAIN = 140 * 1024;

struct Args {
    const void* x; const void* mem0; const void* wq; const void* wk; const void* wv; const void* wo;
    const void* w1; const void* b1; const void* w2; const void* b2; const void* gw; void* out; void* ws;
};
#define TUNE(i, d) (d)

DEV rsrc_t mkrs(const void* p, unsigned n) { return __builtin_amdgcn_make_buffer_rsrc((void*)p, 0, n, 0x00020000); }
DEV u4 ld16(rsrc_t r, unsigned v, unsigned s) { return __builtin_amdgcn_raw_buffer_load_b128(r, v, s, 0); }
DEV u4 ldc16(rsrc_t r, unsigned v, unsigned s) { return __builtin_amdgcn_raw_buffer_load_b128(r, v, s, 16); }
DEV u2 ld8(rsrc_t r, unsigned v, unsigned s) { return __builtin_amdgcn_raw_buffer_load_b64(r, v, s, 0); }
DEV u2 ldc8(rsrc_t r, unsigned v, unsigned s) { return __builtin_amdgcn_raw_buffer_load_b64(r, v, s, 16); }
DEV unsigned ldc4(rsrc_t r, unsigned v, unsigned s) { return __builtin_amdgcn_raw_buffer_load_b32(r, v, s, 16); }
DEV void st16(rsrc_t r, unsigned v, unsigned s, u4 d) { __builtin_amdgcn_raw_buffer_store_b128(d, r, v + s, 0, 0); }
DEV void stc16(rsrc_t r, unsigned v, unsigned s, u4 d) { __builtin_amdgcn_raw_buffer_store_b128(d, r, v + s, 0, 16); }
DEV void stc8(rsrc_t r, unsigned v, unsigned s, u2 d) { __builtin_amdgcn_raw_buffer_store_b64(d, r, v, s, 16); }
DEV void stc4(rsrc_t r, unsigned v, unsigned s, unsigned d) { __builtin_amdgcn_raw_buffer_store_b32(d, r, v, s, 16); }
DEV void stc2(rsrc_t r, unsigned v, unsigned s, unsigned short d) { __builtin_amdgcn_raw_buffer_store_b16(d, r, v, s, 16); }
DEV unsigned pk(float a, float b) { f2 v = {a, b}; return __builtin_bit_cast(unsigned, __builtin_convertvector(v, bf2)); }
DEV float bflo(unsigned d) { return __builtin_bit_cast(float, d << 16); }
DEV float bfhi(unsigned d) { return __builtin_bit_cast(float, d & 0xFFFF0000u); }
DEV f4 mfma(u4 a, u4 b, f4 c) { return __builtin_amdgcn_mfma_f32_16x16x32_bf16(__builtin_bit_cast(bf8, a), __builtin_bit_cast(bf8, b), c, 0, 0, 0); }
DEV f4 smf(u4 a, u8v b, f4 c, int idx) { return __builtin_amdgcn_smfmac_f32_16x16x64_bf16(__builtin_bit_cast(bf8, a), __builtin_bit_cast(bf16v, b), c, idx, 0, 0); }
DEV u8v cat(u4 a, u4 b) { return __builtin_shufflevector(a, b, 0, 1, 2, 3, 4, 5, 6, 7); }
DEV u4 lo4(u8v v) { return __builtin_shufflevector(v, v, 0, 1, 2, 3); }
DEV u4 hi4(u8v v) { return __builtin_shufflevector(v, v, 4, 5, 6, 7); }
DEV void bar() { asm volatile("s_waitcnt lgkmcnt(0)\n\ts_barrier" ::: "memory"); }
DEV void drain() { asm volatile("s_waitcnt vmcnt(0)" ::: "memory"); }
DEV u4 lr16(lptr p) { return *(LAS u4*)p; }
DEV u2 lr8(lptr p) { return *(LAS u2*)p; }
DEV unsigned lr4(lptr p) { return *(LAS unsigned*)p; }
DEV f4 lrf4(lptr p) { return *(LAS f4*)p; }
DEV f2 lrf2(lptr p) { return *(LAS f2*)p; }
DEV void lw16(lptr p, u4 v) { *(LAS u4*)p = v; }
DEV void lwf4(lptr p, f4 v) { *(LAS f4*)p = v; }
DEV float hsum(float a, float b) {
    auto r = __builtin_amdgcn_permlane32_swap(__builtin_bit_cast(unsigned, a), __builtin_bit_cast(unsigned, b), false, false);
    const unsigned r0 = r[0], r1 = r[1];
    return __builtin_bit_cast(float, r0) + __builtin_bit_cast(float, r1);
}
template <int C> DEV float dpp(float v) { return __builtin_bit_cast(float, __builtin_amdgcn_update_dpp(0, __builtin_bit_cast(int, v), C, 0xF, 0xF, true)); }
DEV float max16(float v) { v = fmaxf(v, dpp<0x128>(v)); v = fmaxf(v, dpp<0x124>(v)); v = fmaxf(v, dpp<0x122>(v)); return fmaxf(v, dpp<0x121>(v)); }
DEV float sum16(float v) { v += dpp<0x128>(v); v += dpp<0x124>(v); v += dpp<0x122>(v); return v + dpp<0x121>(v); }
DEV float xr16(float v, bool mx) {
    auto r = __builtin_amdgcn_permlane16_swap(__builtin_bit_cast(unsigned, v), __builtin_bit_cast(unsigned, v), false, false);
    unsigned r0 = r[0], r1 = r[1];
    float a = __builtin_bit_cast(float, r0), b = __builtin_bit_cast(float, r1);
    v = mx ? fmaxf(a, b) : a + b;
    auto q = __builtin_amdgcn_permlane32_swap(__builtin_bit_cast(unsigned, v), __builtin_bit_cast(unsigned, v), false, false);
    r0 = q[0]; r1 = q[1];
    a = __builtin_bit_cast(float, r0); b = __builtin_bit_cast(float, r1);
    return mx ? fmaxf(a, b) : a + b;
}
DEV unsigned umax4(u4 v) { return max(max(v.x, v.y), max(v.z, v.w)); }
DEV float ex2(float v) { return __builtin_amdgcn_exp2f(v); }

DEV int featA(int tau, int c) {
    const int w = tau / 7, t = tau - 7 * w, g = c >> 2, r = c & 3;
    const int u = t < 4 ? w : t < 6 ? 4 + (w >> 1) : 6, p = t < 4 ? t >> 1 : t < 6 ? w & 1 : w >> 1, mb = t < 6 ? t & 1 : w & 1;
    return 64 * u + 16 * g + 8 * p + 4 * (r >> 1) + 2 * mb + (r & 1);
}
DEV int memoff(int f) { return (f >> 6) * 1024 + (2 * ((f >> 4) & 3) + ((f >> 1) & 1)) * 128 + ((f >> 2) & 3) * 4; }

DEV bool spin_fail(rsrc_t ws, unsigned& spins) {
    ++spins;
    if ((spins & 255u) == 0u) {
        if (spins >= SPIN_CAP) { stc4(ws, F_AB, 0, 1u); return true; }
        if (__builtin_amdgcn_readfirstlane(ldc4(ws, F_AB, 0)) != 0u) return true;
    }
    return false;
}
DEV unsigned umax4(u4 v, unsigned m) { return max(max(max(v[0], v[1]), max(v[2], v[3])), m); }
DEV void wait_flags(rsrc_t ws, unsigned off, int n, bool& alive) {
    if (!alive) return;
    const int l = threadIdx.x & 63;
    for (unsigned spins = 0;;) { asm volatile("" ::: "memory");
        unsigned f = ldc4(ws, (l < n ? l : 0) * 4u, off);
        if (__all(f != 0u)) break;
        if (spin_fail(ws, spins)) { alive = false; break; }
        __builtin_amdgcn_s_sleep(4);
    }
    asm volatile("" ::: "memory");
}

constexpr int K0_BLOCKS = 256, LDS_PREP = 57344 + 65536 + 4 * 7680;
DEV void sbar() { __builtin_amdgcn_sched_barrier(0); }
DEV void dma16(rsrc_t r, lptr d, unsigned v, unsigned s) { __builtin_amdgcn_raw_ptr_buffer_load_lds(r, (LAS void*)d, 16, v, s, 0, 0); }
DEV u4 trd(lptr p0, lptr p1) {
    const u2 x0 = __builtin_bit_cast(u2, __builtin_amdgcn_ds_read_tr16_b64_v4i16((LAS s4*)p0));
    const u2 x1 = __builtin_bit_cast(u2, __builtin_amdgcn_ds_read_tr16_b64_v4i16((LAS s4*)p1));
    return u4{x0.x, x0.y, x1.x, x1.y};
}
template <int MAT, int NP>
DEV void prep_rows(rsrc_t ws, lptr lb, lptr la, const u4 (&R)[16], unsigned dst, int l) {
    const int g = l >> 4, c = l & 15;
    u4 A[2][8];
    auto ra = [&](int p, int ke) { return lr16(la + (8 * (c >> 2) + (c & 3) + 4 * p) * 512 + (((4 * ke + g) ^ c) << 4)); };
    auto rb = [&](int i) {
        const int ke = (i % 56) / 7, n = i % 7;
        if (MAT == 0) return lr16(lb + (16 * n + c) * 512 + (((4 * ke + g) ^ c) << 4));
        lptr p0 = lb + (32 * ke + 8 * g + (c >> 2)) * 224 + (16 * n + 4 * (c & 3)) * 2;
        return trd(p0, p0 + 4 * 224);
    };
    u4 B[4];
    A[0][0] = ra(0, 0); A[1][0] = ra(1, 0);
#pragma unroll
    for (int i = 0; i < 3; ++i) B[i] = rb(i);
#pragma unroll
    for (int ke = 1; ke < 8; ++ke) { A[0][ke] = ra(0, ke); A[1][ke] = ra(1, ke); }
    f4 acc[NP][2][7];
    const float sc = MAT ? 1.0f : 1.4426950408889634f / 16.0f;
    auto out = [&](int ps, int n) {
        const f4 x = acc[ps][0][n] * sc, y = acc[ps][1][n] * sc;
        stc16(ws, n * 14336 + ps * 4096 + l * 16, dst, u4{pk(x[0], x[1]), pk(x[2], x[3]), pk(y[0], y[1]), pk(y[2], y[3])});
    };
    const f4 Z = {0, 0, 0, 0};
#pragma unroll
    for (int i = 0; i < 56; ++i) {
        const int ke = i / 7, n = i % 7;
        if (i + 3 < 56 * NP) B[(i + 3) & 3] = rb(i + 3);
        if (NP == 2 && i >= 20 && i < 36) lw16(la + (i - 20) * 1024 + l * 16, R[(i - 20) & 15]);
        if (NP == 2 && i >= 42 && i < 54) A[i & 1][(i - 42) >> 1 & 7] = ra(i & 1, (i - 42) >> 1);
        sbar();
        acc[0][0][n] = mfma(A[0][ke], B[i & 3], ke ? acc[0][0][n] : Z);
        acc[0][1][n] = mfma(A[1][ke], B[i & 3], ke ? acc[0][1][n] : Z);
        sbar();
    }
    if (NP == 2) {
#pragma unroll
        for (int i = 0; i < 56; ++i) {
            const int ke = i / 7, n = i % 7;
            if (i + 3 < 56) B[(i + 3) & 3] = rb(i + 3);
            if (i < 4) A[i & 1][6 + (i >> 1 & 1)] = ra(i & 1, 6 + (i >> 1));
            if (i >= 7 && i < 35 && (i & 3) == 3) out(0, (i - 7) >> 2);
            sbar();
            acc[NP - 1][0][n] = mfma(A[0][ke], B[i & 3], ke ? acc[NP - 1][0][n] : Z);
            acc[NP - 1][1][n] = mfma(A[1][ke], B[i & 3], ke ? acc[NP - 1][1][n] : Z);
            sbar();
        }
    }
#pragma unroll
    for (int n = 0; n < 7; ++n) out(NP - 1, n);
}
template <int MAT>
DEV void prep_ac(const Args& a, rsrc_t ws, lptr lds, int h, int rh, int cq, int w, int l) {
    rsrc_t rA = mkrs(MAT ? a.wv : a.wk, 448 * 4096 * 2), rB = mkrs(MAT ? a.wo : a.wq, 448 * 4096 * 2);
    lptr la = lds + 57344 + w * 16384;
#pragma unroll
    for (int i = 0; i < 14; ++i) {
        const int q = w * 14 + i;
        if (MAT == 0) { const int r = 2 * q + (l >> 5); dma16(rB, lds + q * 1024, featA(7 * cq + (r >> 4), r & 15) * 8192 + (((l & 31) ^ (r & 15)) << 4), h * 512); }
        else { const int u = q * 64 + l, e = u / 14; dma16(rB, lds + q * 1024, e * 896 + (u - 14 * e) * 16, h * 256 * 896 + cq * 224); }
    }
    unsigned vo[4];
#pragma unroll
    for (int i = 0; i < 4; ++i) { const int r = 2 * (i & 1) + (l >> 5) + 8 * (i >> 1); vo[i] = r * 8192 + (((l & 31) ^ ((r & 3) | ((r >> 3) << 2))) << 4); }
    auto aoff = [&](int q) { return (vo[(q & 1) + 2 * ((q >> 2) & 1)] ^ ((q >> 3) << 7)) + ((q >> 1) & 1) * 4 * 8192 + (q >> 3) * 16 * 8192; };
#pragma unroll
    for (int q = 0; q < 16; ++q) dma16(rA, la + q * 1024, aoff(q), (7 * rh + w) * 32 * 8192 + h * 512);
    u4 R[16];
    if (w < 3) {
#pragma unroll
        for (int q = 0; q < 16; ++q) R[q] = ld16(rA, aoff(q), (7 * rh + 4 + w) * 32 * 8192 + h * 512);
        asm volatile("s_waitcnt vmcnt(16)\n\ts_barrier" ::: "memory");
    } else asm volatile("s_waitcnt vmcnt(0)\n\ts_barrier" ::: "memory");
    const unsigned dst = (MAT ? O_CW : O_AW) + ((h * 28 + 7 * cq) * 14 + 7 * rh + w) * 1024;
    if (w < 3) prep_rows<MAT, 2>(ws, lds, la, R, dst, l); else prep_rows<MAT, 1>(ws, lds, la, R, dst, l);
}
__global__ void __launch_bounds__(512, 2) prep_kernel(Args a) {
    extern __shared__ char lds_raw[];
    lptr lds = (lptr)lds_raw;
    const int b = blockIdx.x, t = threadIdx.x, w = __builtin_amdgcn_readfirstlane(t >> 6), l = t & 63, g = l >> 4, c = l & 15;
    rsrc_t ws = mkrs(a.ws, WS_BYTES);
    if (w < 4) {
        const int j = b >> 3, h = 2 * (b & 7) + (j & 1), rh = (j >> 2) & 1, cq = j >> 3;
        if (j & 2) prep_ac<1>(a, ws, lds, h, rh, cq, w, l); else prep_ac<0>(a, ws, lds, h, rh, cq, w, l);
    } else if (w < 6) {
        const int u = 2 * b + w - 4;
        if (u < 448) {
            lptr st = lds + 57344 + 65536 + (w - 4) * 7680;
            const int m2 = u >= 224, q = u - 224 * m2, ng = q / 14, kk = q - 14 * ng;
            rsrc_t rw = mkrs(m2 ? a.w2 : a.w1, 448 * 1792 * 2);
            const int t0 = kk < 12 ? 7 * (kk / 3) + 2 * (kk % 3) : 14 * (kk - 12) + 6, t1 = kk < 12 ? t0 + 1 : t0 + 7;
            u4 v[7];
#pragma unroll
            for (int i = 0; i < 7; ++i) {
                const int z = i * 64 + l, r = z / 14, ch = z - 14 * r;
                v[i] = ld16(rw, m2 ? (448 * (ng >> 2) + 16 * (r < 16 ? t0 : t1) + (r & 15)) * 896 + (ng & 3) * 224 + ch * 16 : (32 * kk + r) * 3584 + ng * 224 + ch * 16, 0);
            }
            asm volatile("s_barrier" ::: "memory");
#pragma unroll
            for (int i = 0; i < 7; ++i) { const int z = i * 64 + l, r = z / 14; lw16(st + r * 240 + (z - 14 * r) * 16, v[i]); }
            lptr p0 = st + ((m2 ? 4 : 8) * g + (c >> 2)) * 240 + (c & 3) * 8;
#pragma unroll
            for (int n = 0; n < 7; ++n) stc16(ws, ((m2 * 112 + ng * 7 + n) * 14 + kk) * 1024 + l * 16, O_W1, trd(p0 + n * 32, p0 + n * 32 + (m2 ? 16 : 4) * 240));
        }
    } else if (w == 6) {
        rsrc_t rx = mkrs(a.x, 2048 * 896), rg = mkrs(a.gw, 896);
        const int m = l & 7, q = l >> 3;
        const unsigned po = ((q & 3) * 16 + (b & 1) * 8 + m) * 16 + (q >> 2) * 1024;
        u4 v[7], gv[7];
        float sum = 0.f, dot = 0.f;
#pragma unroll
        for (int k = 0; k < 7; ++k) { v[k] = ld16(rx, m * 896 + q * 16 + k * 128, b * 8 * 896); gv[k] = ld16(rg, q * 16 + k * 128, 0); }
        asm volatile("s_barrier" ::: "memory");
#pragma unroll
        for (int k = 0; k < 7; ++k) {
            stc16(ws, po + k * 2048, O_XA + (b >> 1) * 14336, v[k]);
#pragma unroll
            for (int d = 0; d < 4; ++d) { sum += bflo(v[k][d]) + bfhi(v[k][d]); dot += bflo(v[k][d]) * bflo(gv[k][d]) + bfhi(v[k][d]) * bfhi(gv[k][d]); }
        }
        const float mean = xr16(sum + __shfl_xor(sum, 8), false) * (1.0f / 448.0f);
        dot = xr16(dot + __shfl_xor(dot, 8), false);
        float var = 0.f;
#pragma unroll
        for (int k = 0; k < 7; ++k)
#pragma unroll
            for (int d = 0; d < 4; ++d) { const float p = bflo(v[k][d]) - mean, r = bfhi(v[k][d]) - mean; var += p * p + r * r; }
        const float rs = 1.0f / sqrtf(xr16(var + __shfl_xor(var, 8), false) * (1.0f / 448.0f) + 1e-5f);
#pragma unroll
        for (int k = 0; k < 7; ++k) {
            u4 y;
#pragma unroll
            for (int d = 0; d < 4; ++d) y[d] = pk((bflo(v[k][d]) - mean) * rs, (bfhi(v[k][d]) - mean) * rs);
            stc16(ws, po + k * 2048, O_YA + (b >> 1) * 14336, y);
        }
        if (l < 8) ((float*)((char*)a.ws + O_GT))[b * 8 + l] = 1.0f / (1.0f + __expf(-dot));
    } else {
        const int i0 = b * 64 + l;
        const unsigned* m0 = (const unsigned*)a.mem0;
        unsigned* wsd = (unsigned*)a.ws;
        if (i0 < 2 * 1792) { const int r = i0 / 1792, i = i0 - 1792 * r, m = i / 224, f = 2 * (i - 224 * m); wsd[(O_MEM + r * 7168 + memoff(f) + m * 16) >> 2] = min(m0[i], 0xFFFFFFFEu); }
        for (unsigned o = O_MEM + MSTR + i0 * 16; o < O_FL; o += 256 * 64 * 16) stc16(ws, o, 0, u4{~0u, ~0u, ~0u, ~0u});
        if (i0 < 2 * 7168) stc16(ws, i0 * 16, O_KM + 62 * 16 * 7168u, u4{~0u, ~0u, ~0u, ~0u});
        if (i0 < 7168 + 4096 + 1024) stc16(ws, i0 < 7168 ? O_WB + 63 * 14 * 8192u + i0 * 16 : i0 < 7168 + 4096 ? O_PX + 62 * 32768u + (i0 - 7168) * 16 : O_PM + 62 * 8192u + (i0 - 11264) * 16, 0, u4{~0u, ~0u, ~0u, ~0u});
        for (unsigned o = O_FL + i0 * 16; o < O_END; o += 256 * 64 * 16) stc16(ws, o, 0, u4{0, 0, 0, 0});
    }
}

#define ASM_S \
    "s_mov_b32 s48, 0\n" \
    "s_mov_b32 s52, 0\n" \
    "s_mov_b32 s59, 0\n" \
    "s_mov_b32 s61, s45\n" \
    "s_mov_b32 s62, 0xff00ff\n" \
    "s_mov_b32 s63, 0xff00ff\n" \
    "s_mov_b32 s88, 0x11111111\n" \
    "s_mov_b32 s89, 0x11111111\n" \
    "s_mov_b32 s90, 0x10001\n" \
    "s_mov_b32 s91, 0x10001\n" \
    "s_lshl_b32 s80, s61, 11\n" \
    "s_add_u32 s61, s61, 1\n" \
    "s_cmp_eq_u32 s61, 7\n" \
    "s_cselect_b32 s61, 0, s61\n" \
    "s_lshl_b32 s81, s61, 11\n" \
    "s_add_u32 s61, s61, 1\n" \
    "s_cmp_eq_u32 s61, 7\n" \
    "s_cselect_b32 s61, 0, s61\n" \
    "s_lshl_b32 s82, s61, 11\n" \
    "s_add_u32 s61, s61, 1\n" \
    "s_cmp_eq_u32 s61, 7\n" \
    "s_cselect_b32 s61, 0, s61\n" \
    "s_lshl_b32 s83, s61, 11\n" \
    "s_add_u32 s61, s61, 1\n" \
    "s_cmp_eq_u32 s61, 7\n" \
    "s_cselect_b32 s61, 0, s61\n" \
    "s_lshl_b32 s84, s61, 11\n" \
    "s_add_u32 s61, s61, 1\n" \
    "s_cmp_eq_u32 s61, 7\n" \
    "s_cselect_b32 s61, 0, s61\n" \
    "s_lshl_b32 s85, s61, 11\n" \
    "s_add_u32 s61, s61, 1\n" \
    "s_cmp_eq_u32 s61, 7\n" \
    "s_cselect_b32 s61, 0, s61\n" \
    "s_lshl_b32 s86, s61, 11\n" \
    "s_add_u32 s61, s61, 1\n" \
    "s_cmp_eq_u32 s61, 7\n" \
    "s_cselect_b32 s61, 0, s61\n" \
    "s_cmp_eq_u32 s46, 4\n" \
    "s_cbranch_scc1 LX_in%=\n" \
    "s_add_u32 s61, s80, s40\n" \
    "buffer_load_dwordx4 a[0:3], v23, s[36:39], s61 offen offset:0\n" \
    "buffer_load_dwordx4 a[4:7], v23, s[36:39], s61 offen offset:1024\n" \
    "s_add_u32 s61, s81, s40\n" \
    "buffer_load_dwordx4 a[8:11], v23, s[36:39], s61 offen offset:0\n" \
    "buffer_load_dwordx4 a[12:15], v23, s[36:39], s61 offen offset:1024\n" \
    "s_add_u32 s61, s82, s40\n" \
    "buffer_load_dwordx4 a[16:19], v23, s[36:39], s61 offen offset:0\n" \
    "buffer_load_dwordx4 a[20:23], v23, s[36:39], s61 offen offset:1024\n" \
    "s_add_u32 s61, s83, s40\n" \
    "buffer_load_dwordx4 a[24:27], v23, s[36:39], s61 offen offset:0\n" \
    "buffer_load_dwordx4 a[28:31], v23, s[36:39], s61 offen offset:1024\n" \
    "s_add_u32 s61, s84, s40\n" \
    "buffer_load_dwordx4 a[32:35], v23, s[36:39], s61 offen offset:0\n" \
    "buffer_load_dwordx4 a[36:39], v23, s[36:39], s61 offen offset:1024\n" \
    "s_add_u32 s61, s85, s40\n" \
    "buffer_load_dwordx4 a[40:43], v23, s[36:39], s61 offen offset:0\n" \
    "buffer_load_dwordx4 a[44:47], v23, s[36:39], s61 offen offset:1024\n" \
    "s_add_u32 s61, s86, s40\n" \
    "buffer_load_dwordx4 a[48:51], v23, s[36:39], s61 offen offset:0\n" \
    "buffer_load_dwordx4 a[52:55], v23, s[36:39], s61 offen offset:1024\n" \
    "s_add_u32 s61, s80, s41\n" \
    "buffer_load_dwordx4 a[56:59], v23, s[36:39], s61 offen offset:0\n" \
    "buffer_load_dwordx4 a[60:63], v23, s[36:39], s61 offen offset:1024\n" \
    "s_add_u32 s61, s81, s41\n" \
    "buffer_load_dwordx4 a[64:67], v23, s[36:39], s61 offen offset:0\n" \
    "buffer_load_dwordx4 a[68:71], v23, s[36:39], s61 offen offset:1024\n" \
    "s_add_u32 s61, s82, s41\n" \
    "buffer_load_dwordx4 a[72:75], v23, s[36:39], s61 offen offset:0\n" \
    "buffer_load_dwordx4 a[76:79], v23, s[36:39], s61 offen offset:1024\n" \
    "s_add_u32 s61, s83, s41\n" \
    "buffer_load_dwordx4 a[80:83], v23, s[36:39], s61 offen offset:0\n" \
    "buffer_load_dwordx4 a[84:87], v23, s[36:39], s61 offen offset:1024\n" \
    "s_add_u32 s61, s84, s41\n" \
    "buffer_load_dwordx4 a[88:91], v23, s[36:39], s61 offen offset:0\n" \
    "buffer_load_dwordx4 a[92:95], v23, s[36:39], s61 offen offset:1024\n" \
    "s_add_u32 s61, s85, s41\n" \
    "buffer_load_dwordx4 a[96:99], v23, s[36:39], s61 offen offset:0\n" \
    "buffer_load_dwordx4 a[100:103], v23, s[36:39], s61 offen offset:1024\n" \
    "s_add_u32 s61, s86, s41\n" \
    "buffer_load_dwordx4 a[104:107], v23, s[36:39], s61 offen offset:0\n" \
    "buffer_load_dwordx4 a[108:111], v23, s[36:39], s61 offen offset:1024\n" \
    "s_add_u32 s61, s80, s42\n" \
    "buffer_load_dwordx4 a[112:115], v23, s[36:39], s61 offen offset:0\n" \
    "buffer_load_dwordx4 a[116:119], v23, s[36:39], s61 offen offset:1024\n" \
    "s_add_u32 s61, s81, s42\n" \
    "buffer_load_dwordx4 a[120:123], v23, s[36:39], s61 offen offset:0\n" \
    "buffer_load_dwordx4 a[124:127], v23, s[36:39], s61 offen offset:1024\n" \
    "s_add_u32 s61, s82, s42\n" \
    "buffer_load_dwordx4 a[128:131], v23, s[36:39], s61 offen offset:0\n" \
    "buffer_load_dwordx4 a[132:135], v23, s[36:39], s61 offen offset:1024\n" \
    "s_add_u32 s61, s83, s42\n" \
    "buffer_load_dwordx4 a[136:139], v23, s[36:39], s61 offen offset:0\n" \
    "buffer_load_dwordx4 a[140:143], v23, s[36:39], s61 offen offset:1024\n" \
    "s_add_u32 s61, s84, s42\n" \
    "buffer_load_dwordx4 a[144:147], v23, s[36:39], s61 offen offset:0\n" \
    "buffer_load_dwordx4 a[148:151], v23, s[36:39], s61 offen offset:1024\n" \
    "s_add_u32 s61, s85, s42\n" \
    "buffer_load_dwordx4 a[152:155], v23, s[36:39], s61 offen offset:0\n" \
    "buffer_load_dwordx4 a[156:159], v23, s[36:39], s61 offen offset:1024\n" \
    "s_add_u32 s61, s86, s42\n" \
    "buffer_load_dwordx4 a[160:163], v23, s[36:39], s61 offen offset:0\n" \
    "buffer_load_dwordx4 a[164:167], v23, s[36:39], s61 offen offset:1024\n" \
    "s_add_u32 s61, s80, s43\n" \
    "buffer_load_dwordx4 a[168:171], v23, s[36:39], s61 offen offset:0\n" \
    "buffer_load_dwordx4 a[172:175], v23, s[36:39], s61 offen offset:1024\n" \
    "s_add_u32 s61, s81, s43\n" \
    "buffer_load_dwordx4 a[176:179], v23, s[36:39], s61 offen offset:0\n" \
    "buffer_load_dwordx4 a[180:183], v23, s[36:39], s61 offen offset:1024\n" \
    "s_add_u32 s61, s82, s43\n" \
    "buffer_load_dwordx4 a[184:187], v23, s[36:39], s61 offen offset:0\n" \
    "buffer_load_dwordx4 a[188:191], v23, s[36:39], s61 offen offset:1024\n" \
    "s_add_u32 s61, s83, s43\n" \
    "buffer_load_dwordx4 a[192:195], v23, s[36:39], s61 offen offset:0\n" \
    "buffer_load_dwordx4 a[196:199], v23, s[36:39], s61 offen offset:1024\n" \
    "s_add_u32 s61, s84, s43\n" \
    "buffer_load_dwordx4 a[200:203], v23, s[36:39], s61 offen offset:0\n" \
    "buffer_load_dwordx4 a[204:207], v23, s[36:39], s61 offen offset:1024\n" \
    "s_add_u32 s61, s85, s43\n" \
    "buffer_load_dwordx4 a[208:211], v23, s[36:39], s61 offen offset:0\n" \
    "buffer_load_dwordx4 a[212:215], v23, s[36:39], s61 offen offset:1024\n" \
    "s_add_u32 s61, s86, s43\n" \
    "buffer_load_dwordx4 a[216:219], v23, s[36:39], s61 offen offset:0\n" \
    "buffer_load_dwordx4 a[220:223], v23, s[36:39], s61 offen offset:1024\n" \
    "s_waitcnt vmcnt(0)\n" \
    "s_memtime s[56:57]\n" \
    "s_waitcnt lgkmcnt(0)\n" \
    "s_sub_u32 s56, s56, s92\n" \
    "LS_top%=:\n" \
    "v_mov_b64 v[104:105], 0\n" \
    "v_mov_b64 v[106:107], 0\n" \
    "v_mov_b64 v[76:77], 0\n" \
    "v_mov_b64 v[78:79], 0\n" \
    "v_mov_b64 v[80:81], 0\n" \
    "v_mov_b64 v[82:83], 0\n" \
    "v_mov_b64 v[84:85], 0\n" \
    "v_mov_b64 v[86:87], 0\n" \
    "v_mov_b64 v[88:89], 0\n" \
    "v_mov_b64 v[90:91], 0\n" \
    "LS_w%=:\n" \
    "s_memtime s[54:55]\n" \
    "s_waitcnt lgkmcnt(0)\n" \
    "s_sub_u32 s57, s54, s56\n" \
    "s_cmp_lt_i32 s57, s92\n" \
    "s_cbranch_scc1 LS_w%=\n" \
    "buffer_load_dwordx4 v[32:35], v28, s[36:39], s49 offen sc1\n" \
    "buffer_load_dwordx4 v[36:39], v29, s[36:39], s49 offen sc1\n" \
    "s_sleep 5\n" \
    "s_nop 6\n" \
    "s_add_u32 s61, s49, 7168\n" \
    "buffer_load_dwordx4 v[60:63], v28, s[36:39], s61 offen sc1\n" \
    "buffer_load_dwordx4 v[64:67], v29, s[36:39], s61 offen sc1\n" \
    "LS_p0%=:\n" \
    "s_waitcnt vmcnt(2)\n" \
    "v_max3_u32 v68, v32, v33, v34\n" \
    "v_max3_u32 v69, v35, v36, v37\n" \
    "v_max3_u32 v68, v68, v38, v39\n" \
    "v_max_u32 v68, v68, v69\n" \
    "v_cmp_eq_u32 vcc, -1, v68\n" \
    "s_cbranch_vccnz LS_r0%=\n" \
    "ds_write_b128 v0, v[32:35]\n" \
    "ds_write_b128 v1, v[36:39]\n" \
    "v_smfmac_f32_16x16x64_bf16 v[76:79], v[32:35], a[0:7], v8\n" \
    "v_smfmac_f32_16x16x64_bf16 v[80:83], v[32:35], a[56:63], v8\n" \
    "v_smfmac_f32_16x16x64_bf16 v[84:87], v[32:35], a[112:119], v8\n" \
    "v_smfmac_f32_16x16x64_bf16 v[88:91], v[32:35], a[168:175], v8\n" \
    "v_smfmac_f32_16x16x64_bf16 v[76:79], v[36:39], a[8:15], v8\n" \
    "s_waitcnt lgkmcnt(0)\n" \
    "s_barrier\n" \
    "ds_read_b128 v[40:43], v2\n" \
    "ds_read_b128 v[44:47], v3\n" \
    "v_smfmac_f32_16x16x64_bf16 v[80:83], v[36:39], a[64:71], v8\n" \
    "ds_read_b128 v[48:51], v4\n" \
    "ds_read_b128 v[52:55], v5\n" \
    "ds_read_b128 v[56:59], v6\n" \
    "v_smfmac_f32_16x16x64_bf16 v[84:87], v[36:39], a[120:127], v8\n" \
    "v_smfmac_f32_16x16x64_bf16 v[88:91], v[36:39], a[176:183], v8\n" \
    "LS_c%=:\n" \
    "s_waitcnt lgkmcnt(4)\n" \
    "v_smfmac_f32_16x16x64_bf16 v[76:79], v[40:43], a[16:23], v8\n" \
    "v_smfmac_f32_16x16x64_bf16 v[80:83], v[40:43], a[72:79], v8\n" \
    "s_waitcnt lgkmcnt(3)\n" \
    "v_smfmac_f32_16x16x64_bf16 v[76:79], v[44:47], a[24:31], v8\n" \
    "v_smfmac_f32_16x16x64_bf16 v[80:83], v[44:47], a[80:87], v8\n" \
    "s_waitcnt lgkmcnt(2)\n" \
    "v_smfmac_f32_16x16x64_bf16 v[76:79], v[48:51], a[32:39], v8\n" \
    "v_smfmac_f32_16x16x64_bf16 v[80:83], v[48:51], a[88:95], v8\n" \
    "s_waitcnt lgkmcnt(1)\n" \
    "v_smfmac_f32_16x16x64_bf16 v[76:79], v[52:55], a[40:47], v8\n" \
    "v_smfmac_f32_16x16x64_bf16 v[80:83], v[52:55], a[96:103], v8\n" \
    "s_waitcnt lgkmcnt(0)\n" \
    "v_smfmac_f32_16x16x64_bf16 v[76:79], v[56:59], a[48:55], v8\n" \
    "v_smfmac_f32_16x16x64_bf16 v[80:83], v[56:59], a[104:111], v8\n" \
    "v_smfmac_f32_16x16x64_bf16 v[84:87], v[40:43], a[128:135], v8\n" \
    "ds_read2_b32 v[92:93], v12 offset0:0 offset1:32\n" \
    "ds_read2_b32 v[94:95], v12 offset0:1 offset1:33\n" \
    "v_smfmac_f32_16x16x64_bf16 v[88:91], v[40:43], a[184:191], v8\n" \
    "ds_read2_b32 v[96:97], v13 offset0:0 offset1:32\n" \
    "ds_read2_b32 v[98:99], v13 offset0:1 offset1:33\n" \
    "v_smfmac_f32_16x16x64_bf16 v[84:87], v[44:47], a[136:143], v8\n" \
    "v_cvt_pk_bf16_f32 v68, v76, v77\n" \
    "v_cvt_pk_bf16_f32 v69, v78, v79\n" \
    "v_smfmac_f32_16x16x64_bf16 v[88:91], v[44:47], a[192:199], v8\n" \
    "v_cvt_pk_bf16_f32 v70, v80, v81\n" \
    "v_cvt_pk_bf16_f32 v71, v82, v83\n" \
    "v_smfmac_f32_16x16x64_bf16 v[84:87], v[48:51], a[144:151], v8\n" \
    "v_mfma_f32_16x16x32_bf16 v[72:75], v[68:71], v[24:27], 0\n" \
    "v_smfmac_f32_16x16x64_bf16 v[88:91], v[48:51], a[200:207], v8\n" \
    "v_smfmac_f32_16x16x64_bf16 v[84:87], v[52:55], a[152:159], v8\n" \
    "v_smfmac_f32_16x16x64_bf16 v[88:91], v[52:55], a[208:215], v8\n" \
    "v_cvt_pk_bf16_f32 v100, v72, v73\n" \
    "v_cvt_pk_bf16_f32 v101, v74, v75\n" \
    "v_smfmac_f32_16x16x64_bf16 v[84:87], v[56:59], a[160:167], v8\n" \
    "v_smfmac_f32_16x16x64_bf16 v[88:91], v[56:59], a[216:223], v8\n" \
    "s_nop 4\n" \
    "v_cvt_pk_bf16_f32 v68, v84, v85\n" \
    "v_cvt_pk_bf16_f32 v69, v86, v87\n" \
    "s_nop 0\n" \
    "v_cvt_pk_bf16_f32 v70, v88, v89\n" \
    "v_cvt_pk_bf16_f32 v71, v90, v91\n" \
    "s_nop 1\n" \
    "v_mfma_f32_16x16x32_bf16 v[72:75], v[68:71], v[24:27], 0\n" \
    "s_nop 7\n" \
    "v_cvt_pk_bf16_f32 v102, v72, v73\n" \
    "v_cvt_pk_bf16_f32 v103, v74, v75\n" \
    "s_waitcnt lgkmcnt(0)\n" \
    "s_nop 0\n" \
    "v_smfmac_f32_16x16x64_bf16 v[104:107], v[100:103], v[92:99], v8\n" \
    "s_mov_b64 exec, s[62:63]\n" \
    "s_nop 6\n" \
    "ds_write_b128 v18, v[104:107]\n" \
    "s_mov_b64 exec, -1\n" \
    "s_cmp_eq_u32 s48, 63\n" \
    "s_cbranch_scc1 LS_last%=\n" \
    "s_waitcnt lgkmcnt(0)\n" \
    "s_barrier\n" \
    "s_cmp_lg_u32 s46, 0\n" \
    "s_cbranch_scc1 LS_idle%=\n" \
    "ds_read_b128 v[108:111], v19\n" \
    "ds_read_b128 v[112:115], v19 offset:256\n" \
    "s_waitcnt lgkmcnt(0)\n" \
    "v_pk_add_f32 v[108:109], v[108:109], v[112:113]\n" \
    "v_pk_add_f32 v[110:111], v[110:111], v[114:115]\n" \
    "s_nop 1\n" \
    "v_permlane32_swap_b32 v108, v110\n" \
    "v_permlane32_swap_b32 v109, v111\n" \
    "v_pk_add_f32 v[108:109], v[108:109], v[110:111]\n" \
    "v_mov_b64 v[110:111], v[108:109]\n" \
    "s_nop 1\n" \
    "v_permlane16_swap_b32 v108, v110\n" \
    "v_permlane16_swap_b32 v109, v111\n" \
    "v_pk_add_f32 v[108:109], v[108:109], v[110:111]\n" \
    "v_mov_b64 v[110:111], v[108:109]\n" \
    "s_nop 1\n" \
    "v_permlane32_swap_b32 v108, v110\n" \
    "v_permlane32_swap_b32 v109, v111\n" \
    "s_mov_b32 exec_hi, 0\n" \
    "buffer_store_dwordx4 v[108:111], v20, s[36:39], s51 offen sc1\n" \
    "s_mov_b64 exec, -1\n" \
    "s_branch LS_j%=\n" \
    "LS_idle%=:\n" \
    "s_sleep 2\n" \
    "LS_j%=:\n" \
    "s_memtime s[56:57]\n" \
    "s_waitcnt lgkmcnt(0)\n" \
    "s_add_u32 s56, s56, s59\n" \
    "s_mov_b32 s59, 0\n" \
    "s_sleep 7\n" \
    "s_nop 12\n" \
    "buffer_store_dwordx4 v[100:103], v21, s[36:39], s50 offen sc1\n" \
    "s_add_u32 s50, s50, 114688\n" \
    "s_add_u32 s49, s49, 14336\n" \
    "s_add_u32 s51, s51, 25600\n" \
    "s_add_u32 s48, s48, 1\n" \
    "s_branch LS_top%=\n" \
    "LS_last%=:\n" \
    "buffer_store_dwordx4 v[100:103], v21, s[36:39], s50 offen sc1\n" \
    "s_add_u32 s50, s50, 114688\n" \
    "s_waitcnt vmcnt(0)\n" \
    "s_barrier\n" \
    "s_cmp_lg_u32 s46, 0\n" \
    "s_cbranch_scc1 LS_end%=\n" \
    "s_mov_b64 exec, 1\n" \
    "v_mov_b32 v76, 1\n" \
    "v_mov_b32 v77, s47\n" \
    "buffer_store_dword v76, v77, s[36:39], 0 offen sc1\n" \
    "s_mov_b64 exec, -1\n" \
    "s_branch LS_end%=\n" \
    "LS_r0%=:\n" \
    "s_add_u32 s52, s52, 1\n" \
    "s_cmp_ge_u32 s52, 2097152\n" \
    "s_cbranch_scc1 LS_ab%=\n" \
    "s_max_u32 s59, s59, s94\n" \
    "buffer_load_dwordx4 v[32:35], v28, s[36:39], s49 offen sc1\n" \
    "buffer_load_dwordx4 v[36:39], v29, s[36:39], s49 offen sc1\n" \
    "s_waitcnt vmcnt(2)\n" \
    "v_max3_u32 v68, v60, v61, v62\n" \
    "v_max3_u32 v69, v63, v64, v65\n" \
    "v_max3_u32 v68, v68, v66, v67\n" \
    "v_max_u32 v68, v68, v69\n" \
    "v_cmp_eq_u32 vcc, -1, v68\n" \
    "s_cbranch_vccnz LS_r1%=\n" \
    "ds_write_b128 v0, v[60:63]\n" \
    "ds_write_b128 v1, v[64:67]\n" \
    "v_smfmac_f32_16x16x64_bf16 v[76:79], v[60:63], a[0:7], v8\n" \
    "v_smfmac_f32_16x16x64_bf16 v[80:83], v[60:63], a[56:63], v8\n" \
    "v_smfmac_f32_16x16x64_bf16 v[84:87], v[60:63], a[112:119], v8\n" \
    "v_smfmac_f32_16x16x64_bf16 v[88:91], v[60:63], a[168:175], v8\n" \
    "v_smfmac_f32_16x16x64_bf16 v[76:79], v[64:67], a[8:15], v8\n" \
    "s_waitcnt lgkmcnt(0)\n" \
    "s_barrier\n" \
    "ds_read_b128 v[40:43], v2\n" \
    "ds_read_b128 v[44:47], v3\n" \
    "v_smfmac_f32_16x16x64_bf16 v[80:83], v[64:67], a[64:71], v8\n" \
    "ds_read_b128 v[48:51], v4\n" \
    "ds_read_b128 v[52:55], v5\n" \
    "ds_read_b128 v[56:59], v6\n" \
    "v_smfmac_f32_16x16x64_bf16 v[84:87], v[64:67], a[120:127], v8\n" \
    "v_smfmac_f32_16x16x64_bf16 v[88:91], v[64:67], a[176:183], v8\n" \
    "s_branch LS_c%=\n" \
    "LS_r1%=:\n" \
    "s_add_u32 s52, s52, 1\n" \
    "s_cmp_ge_u32 s52, 2097152\n" \
    "s_cbranch_scc1 LS_ab%=\n" \
    "s_max_u32 s59, s59, 0\n" \
    "s_add_u32 s61, s49, 7168\n" \
    "buffer_load_dwordx4 v[60:63], v28, s[36:39], s61 offen sc1\n" \
    "buffer_load_dwordx4 v[64:67], v29, s[36:39], s61 offen sc1\n" \
    "s_branch LS_p0%=\n" \
    "LX_in%=:\n" \
    "buffer_load_dword v92, v30, s[36:39], s41 offen sc1\n" \
    "s_waitcnt vmcnt(0)\n" \
    "LXA%=:\n" \
    "v_cmp_eq_u32 vcc, 0, v92\n" \
    "s_nop 4\n" \
    "s_cbranch_vccz LXAk%=\n" \
    "s_add_u32 s52, s52, 1\n" \
    "s_cmp_ge_u32 s52, 2097152\n" \
    "s_cbranch_scc1 LS_ab%=\n" \
    "buffer_load_dword v92, v30, s[36:39], s41 offen sc1\n" \
    "s_waitcnt vmcnt(0)\n" \
    "s_branch LXA%=\n" \
    "LXAk%=:\n" \
    "s_add_u32 s61, s80, s40\n" \
    "buffer_load_dwordx4 a[0:3], v23, s[36:39], s61 offen offset:0 sc1\n" \
    "buffer_load_dwordx4 a[4:7], v23, s[36:39], s61 offen offset:1024 sc1\n" \
    "s_add_u32 s61, s81, s40\n" \
    "buffer_load_dwordx4 a[8:11], v23, s[36:39], s61 offen offset:0 sc1\n" \
    "buffer_load_dwordx4 a[12:15], v23, s[36:39], s61 offen offset:1024 sc1\n" \
    "s_add_u32 s61, s82, s40\n" \
    "buffer_load_dwordx4 a[16:19], v23, s[36:39], s61 offen offset:0 sc1\n" \
    "buffer_load_dwordx4 a[20:23], v23, s[36:39], s61 offen offset:1024 sc1\n" \
    "s_add_u32 s61, s83, s40\n" \
    "buffer_load_dwordx4 a[24:27], v23, s[36:39], s61 offen offset:0 sc1\n" \
    "buffer_load_dwordx4 a[28:31], v23, s[36:39], s61 offen offset:1024 sc1\n" \
    "s_add_u32 s61, s84, s40\n" \
    "buffer_load_dwordx4 a[32:35], v23, s[36:39], s61 offen offset:0 sc1\n" \
    "buffer_load_dwordx4 a[36:39], v23, s[36:39], s61 offen offset:1024 sc1\n" \
    "s_add_u32 s61, s85, s40\n" \
    "buffer_load_dwordx4 a[40:43], v23, s[36:39], s61 offen offset:0 sc1\n" \
    "buffer_load_dwordx4 a[44:47], v23, s[36:39], s61 offen offset:1024 sc1\n" \
    "s_add_u32 s61, s86, s40\n" \
    "buffer_load_dwordx4 a[48:51], v23, s[36:39], s61 offen offset:0 sc1\n" \
    "buffer_load_dwordx4 a[52:55], v23, s[36:39], s61 offen offset:1024 sc1\n" \
    "s_add_u32 s40, s40, 14336\n" \
    "s_add_u32 s61, s80, s40\n" \
    "buffer_load_dwordx4 a[56:59], v23, s[36:39], s61 offen offset:0 sc1\n" \
    "buffer_load_dwordx4 a[60:63], v23, s[36:39], s61 offen offset:1024 sc1\n" \
    "s_add_u32 s61, s81, s40\n" \
    "buffer_load_dwordx4 a[64:67], v23, s[36:39], s61 offen offset:0 sc1\n" \
    "buffer_load_dwordx4 a[68:71], v23, s[36:39], s61 offen offset:1024 sc1\n" \
    "s_add_u32 s61, s82, s40\n" \
    "buffer_load_dwordx4 a[72:75], v23, s[36:39], s61 offen offset:0 sc1\n" \
    "buffer_load_dwordx4 a[76:79], v23, s[36:39], s61 offen offset:1024 sc1\n" \
    "s_add_u32 s61, s83, s40\n" \
    "buffer_load_dwordx4 a[80:83], v23, s[36:39], s61 offen offset:0 sc1\n" \
    "buffer_load_dwordx4 a[84:87], v23, s[36:39], s61 offen offset:1024 sc1\n" \
    "s_add_u32 s61, s84, s40\n" \
    "buffer_load_dwordx4 a[88:91], v23, s[36:39], s61 offen offset:0 sc1\n" \
    "buffer_load_dwordx4 a[92:95], v23, s[36:39], s61 offen offset:1024 sc1\n" \
    "s_add_u32 s61, s85, s40\n" \
    "buffer_load_dwordx4 a[96:99], v23, s[36:39], s61 offen offset:0 sc1\n" \
    "buffer_load_dwordx4 a[100:103], v23, s[36:39], s61 offen offset:1024 sc1\n" \
    "s_add_u32 s61, s86, s40\n" \
    "buffer_load_dwordx4 a[104:107], v23, s[36:39], s61 offen offset:0 sc1\n" \
    "buffer_load_dwordx4 a[108:111], v23, s[36:39], s61 offen offset:1024 sc1\n" \
    "s_add_u32 s40, s40, 444416\n" \
    "s_waitcnt vmcnt(0)\n" \
    "s_memtime s[56:57]\n" \
    "s_waitcnt lgkmcnt(0)\n" \
    "s_sub_u32 s56, s56, s92\n" \
    "LX_top%=:\n" \
    "s_and_b32 s61, s48, 1\n" \
    "s_lshl_b32 s61, s61, 8\n" \
    "s_add_u32 s41, s41, s61\n" \
    "buffer_load_dword v92, v30, s[36:39], s41 offen sc1\n" \
    "v_mov_b64 v[76:77], 0\n" \
    "v_mov_b64 v[78:79], 0\n" \
    "v_mov_b64 v[80:81], 0\n" \
    "v_mov_b64 v[82:83], 0\n" \
    "LX_w%=:\n" \
    "s_memtime s[54:55]\n" \
    "s_waitcnt lgkmcnt(0)\n" \
    "s_sub_u32 s57, s54, s56\n" \
    "s_cmp_lt_i32 s57, s92\n" \
    "s_cbranch_scc1 LX_w%=\n" \
    "buffer_load_dwordx4 v[32:35], v28, s[36:39], s49 offen sc1\n" \
    "buffer_load_dwordx4 v[36:39], v29, s[36:39], s49 offen sc1\n" \
    "s_sleep 5\n" \
    "s_nop 6\n" \
    "s_add_u32 s61, s49, 7168\n" \
    "buffer_load_dwordx4 v[60:63], v28, s[36:39], s61 offen sc1\n" \
    "buffer_load_dwordx4 v[64:67], v29, s[36:39], s61 offen sc1\n" \
    "LX_p0%=:\n" \
    "s_waitcnt vmcnt(2)\n" \
    "v_max3_u32 v68, v32, v33, v34\n" \
    "v_max3_u32 v69, v35, v36, v37\n" \
    "v_max3_u32 v68, v68, v38, v39\n" \
    "v_max_u32 v68, v68, v69\n" \
    "v_cmp_eq_u32 vcc, -1, v68\n" \
    "s_cbranch_vccnz LX_r0%=\n" \
    "ds_write_b128 v0, v[32:35]\n" \
    "ds_write_b128 v1, v[36:39]\n" \
    "v_smfmac_f32_16x16x64_bf16 v[76:79], v[32:35], a[0:7], v8\n" \
    "v_smfmac_f32_16x16x64_bf16 v[80:83], v[32:35], a[56:63], v8\n" \
    "v_smfmac_f32_16x16x64_bf16 v[76:79], v[36:39], a[8:15], v8\n" \
    "v_smfmac_f32_16x16x64_bf16 v[80:83], v[36:39], a[64:71], v8\n" \
    "s_waitcnt lgkmcnt(0)\n" \
    "s_barrier\n" \
    "ds_read_b128 v[40:43], v2\n" \
    "ds_read_b128 v[44:47], v3\n" \
    "ds_read_b128 v[48:51], v4\n" \
    "ds_read_b128 v[52:55], v5\n" \
    "ds_read_b128 v[56:59], v6\n" \
    "LX_c%=:\n" \
    "s_cmp_eq_u32 s48, 63\n" \
    "s_cbranch_scc1 LX_last%=\n" \
    "s_waitcnt lgkmcnt(4)\n" \
    "v_smfmac_f32_16x16x64_bf16 v[76:79], v[40:43], a[16:23], v8\n" \
    "v_smfmac_f32_16x16x64_bf16 v[80:83], v[40:43], a[72:79], v8\n" \
    "s_waitcnt lgkmcnt(3)\n" \
    "v_smfmac_f32_16x16x64_bf16 v[76:79], v[44:47], a[24:31], v8\n" \
    "v_smfmac_f32_16x16x64_bf16 v[80:83], v[44:47], a[80:87], v8\n" \
    "s_waitcnt lgkmcnt(2)\n" \
    "v_smfmac_f32_16x16x64_bf16 v[76:79], v[48:51], a[32:39], v8\n" \
    "v_smfmac_f32_16x16x64_bf16 v[80:83], v[48:51], a[88:95], v8\n" \
    "s_waitcnt lgkmcnt(1)\n" \
    "v_smfmac_f32_16x16x64_bf16 v[76:79], v[52:55], a[40:47], v8\n" \
    "v_smfmac_f32_16x16x64_bf16 v[80:83], v[52:55], a[96:103], v8\n" \
    "s_waitcnt lgkmcnt(0)\n" \
    "v_smfmac_f32_16x16x64_bf16 v[76:79], v[56:59], a[48:55], v8\n" \
    "v_smfmac_f32_16x16x64_bf16 v[80:83], v[56:59], a[104:111], v8\n" \
    "s_nop 4\n" \
    "v_permlane32_swap_b32 v76, v77\n" \
    "v_permlane32_swap_b32 v78, v79\n" \
    "v_add_f32 v108, v76, v77\n" \
    "v_add_f32 v110, v78, v79\n" \
    "v_permlane32_swap_b32 v80, v81\n" \
    "v_permlane32_swap_b32 v82, v83\n" \
    "v_add_f32 v109, v80, v81\n" \
    "v_add_f32 v111, v82, v83\n" \
    "v_max_f32 v112, v108, v109\n" \
    "v_max_f32 v114, v110, v111\n" \
    "s_nop 0\n" \
    "v_max_f32_dpp v112, v112, v112 row_ror:8 row_mask:0xf bank_mask:0xf\n" \
    "v_max_f32_dpp v114, v114, v114 row_ror:8 row_mask:0xf bank_mask:0xf\n" \
    "s_nop 0\n" \
    "v_max_f32_dpp v112, v112, v112 row_ror:4 row_mask:0xf bank_mask:0xf\n" \
    "v_max_f32_dpp v114, v114, v114 row_ror:4 row_mask:0xf bank_mask:0xf\n" \
    "s_nop 0\n" \
    "v_max_f32_dpp v112, v112, v112 row_ror:2 row_mask:0xf bank_mask:0xf\n" \
    "v_max_f32_dpp v114, v114, v114 row_ror:2 row_mask:0xf bank_mask:0xf\n" \
    "s_nop 0\n" \
    "v_max_f32_dpp v112, v112, v112 row_ror:1 row_mask:0xf bank_mask:0xf\n" \
    "v_max_f32_dpp v114, v114, v114 row_ror:1 row_mask:0xf bank_mask:0xf\n" \
    "v_sub_f32 v108, v108, v112\n" \
    "v_sub_f32 v109, v109, v112\n" \
    "v_sub_f32 v110, v110, v114\n" \
    "v_sub_f32 v111, v111, v114\n" \
    "v_exp_f32 v108, v108\n" \
    "v_exp_f32 v109, v109\n" \
    "v_exp_f32 v110, v110\n" \
    "v_exp_f32 v111, v111\n" \
    "v_cvt_pk_bf16_f32 v116, v108, v109\n" \
    "v_cvt_pk_bf16_f32 v120, v110, v111\n" \
    "v_add_f32 v113, v108, v109\n" \
    "v_add_f32 v115, v110, v111\n" \
    "v_mov_b32_dpp v117, v116 quad_perm:[1,1,1,1] row_mask:0xf bank_mask:0xf\n" \
    "v_mov_b32_dpp v121, v120 quad_perm:[1,1,1,1] row_mask:0xf bank_mask:0xf\n" \
    "v_mov_b32_dpp v118, v116 quad_perm:[2,2,2,2] row_mask:0xf bank_mask:0xf\n" \
    "v_mov_b32_dpp v122, v120 quad_perm:[2,2,2,2] row_mask:0xf bank_mask:0xf\n" \
    "v_mov_b32_dpp v119, v116 quad_perm:[3,3,3,3] row_mask:0xf bank_mask:0xf\n" \
    "v_mov_b32_dpp v123, v120 quad_perm:[3,3,3,3] row_mask:0xf bank_mask:0xf\n" \
    "s_mov_b64 exec, s[88:89]\n" \
    "buffer_store_dwordx4 v[116:119], v20, s[36:39], s51 offen sc1\n" \
    "buffer_store_dwordx4 v[120:123], v20, s[36:39], s51 offen offset:256 sc1\n" \
    "s_mov_b64 exec, -1\n" \
    "v_add_f32_dpp v113, v113, v113 row_ror:8 row_mask:0xf bank_mask:0xf\n" \
    "v_add_f32_dpp v115, v115, v115 row_ror:8 row_mask:0xf bank_mask:0xf\n" \
    "s_nop 0\n" \
    "v_add_f32_dpp v113, v113, v113 row_ror:4 row_mask:0xf bank_mask:0xf\n" \
    "v_add_f32_dpp v115, v115, v115 row_ror:4 row_mask:0xf bank_mask:0xf\n" \
    "s_nop 0\n" \
    "v_add_f32_dpp v113, v113, v113 row_ror:2 row_mask:0xf bank_mask:0xf\n" \
    "v_add_f32_dpp v115, v115, v115 row_ror:2 row_mask:0xf bank_mask:0xf\n" \
    "s_nop 0\n" \
    "v_add_f32_dpp v113, v113, v113 row_ror:1 row_mask:0xf bank_mask:0xf\n" \
    "v_add_f32_dpp v115, v115, v115 row_ror:1 row_mask:0xf bank_mask:0xf\n" \
    "s_mov_b64 exec, s[90:91]\n" \
    "buffer_store_dwordx4 v[112:115], v21, s[36:39], s51 offen sc1\n" \
    "s_mov_b64 exec, -1\n" \
    "s_barrier\n" \
    "s_sleep 2\n" \
    "s_memtime s[56:57]\n" \
    "s_waitcnt lgkmcnt(0)\n" \
    "s_add_u32 s56, s56, s59\n" \
    "s_mov_b32 s59, 0\n" \
    "s_sleep 6\n" \
    "s_nop 3\n" \
    "LXB%=:\n" \
    "v_cmp_eq_u32 vcc, 0, v92\n" \
    "s_nop 4\n" \
    "s_cbranch_vccz LXBk%=\n" \
    "s_add_u32 s52, s52, 1\n" \
    "s_cmp_ge_u32 s52, 2097152\n" \
    "s_cbranch_scc1 LS_ab%=\n" \
    "buffer_load_dword v92, v30, s[36:39], s41 offen sc1\n" \
    "s_waitcnt vmcnt(0)\n" \
    "s_branch LXB%=\n" \
    "LXBk%=:\n" \
    "s_add_u32 s61, s80, s40\n" \
    "buffer_load_dwordx4 a[0:3], v23, s[36:39], s61 offen offset:0 sc1\n" \
    "buffer_load_dwordx4 a[4:7], v23, s[36:39], s61 offen offset:1024 sc1\n" \
    "s_add_u32 s61, s81, s40\n" \
    "buffer_load_dwordx4 a[8:11], v23, s[36:39], s61 offen offset:0 sc1\n" \
    "buffer_load_dwordx4 a[12:15], v23, s[36:39], s61 offen offset:1024 sc1\n" \
    "s_add_u32 s61, s82, s40\n" \
    "buffer_load_dwordx4 a[16:19], v23, s[36:39], s61 offen offset:0 sc1\n" \
    "buffer_load_dwordx4 a[20:23], v23, s[36:39], s61 offen offset:1024 sc1\n" \
    "s_add_u32 s61, s83, s40\n" \
    "buffer_load_dwordx4 a[24:27], v23, s[36:39], s61 offen offset:0 sc1\n" \
    "buffer_load_dwordx4 a[28:31], v23, s[36:39], s61 offen offset:1024 sc1\n" \
    "s_add_u32 s61, s84, s40\n" \
    "buffer_load_dwordx4 a[32:35], v23, s[36:39], s61 offen offset:0 sc1\n" \
    "buffer_load_dwordx4 a[36:39], v23, s[36:39], s61 offen offset:1024 sc1\n" \
    "s_add_u32 s61, s85, s40\n" \
    "buffer_load_dwordx4 a[40:43], v23, s[36:39], s61 offen offset:0 sc1\n" \
    "buffer_load_dwordx4 a[44:47], v23, s[36:39], s61 offen offset:1024 sc1\n" \
    "s_add_u32 s61, s86, s40\n" \
    "buffer_load_dwordx4 a[48:51], v23, s[36:39], s61 offen offset:0 sc1\n" \
    "buffer_load_dwordx4 a[52:55], v23, s[36:39], s61 offen offset:1024 sc1\n" \
    "s_add_u32 s40, s40, 14336\n" \
    "s_add_u32 s61, s80, s40\n" \
    "buffer_load_dwordx4 a[56:59], v23, s[36:39], s61 offen offset:0 sc1\n" \
    "buffer_load_dwordx4 a[60:63], v23, s[36:39], s61 offen offset:1024 sc1\n" \
    "s_add_u32 s61, s81, s40\n" \
    "buffer_load_dwordx4 a[64:67], v23, s[36:39], s61 offen offset:0 sc1\n" \
    "buffer_load_dwordx4 a[68:71], v23, s[36:39], s61 offen offset:1024 sc1\n" \
    "s_add_u32 s61, s82, s40\n" \
    "buffer_load_dwordx4 a[72:75], v23, s[36:39], s61 offen offset:0 sc1\n" \
    "buffer_load_dwordx4 a[76:79], v23, s[36:39], s61 offen offset:1024 sc1\n" \
    "s_add_u32 s61, s83, s40\n" \
    "buffer_load_dwordx4 a[80:83], v23, s[36:39], s61 offen offset:0 sc1\n" \
    "buffer_load_dwordx4 a[84:87], v23, s[36:39], s61 offen offset:1024 sc1\n" \
    "s_add_u32 s61, s84, s40\n" \
    "buffer_load_dwordx4 a[88:91], v23, s[36:39], s61 offen offset:0 sc1\n" \
    "buffer_load_dwordx4 a[92:95], v23, s[36:39], s61 offen offset:1024 sc1\n" \
    "s_add_u32 s61, s85, s40\n" \
    "buffer_load_dwordx4 a[96:99], v23, s[36:39], s61 offen offset:0 sc1\n" \
    "buffer_load_dwordx4 a[100:103], v23, s[36:39], s61 offen offset:1024 sc1\n" \
    "s_add_u32 s61, s86, s40\n" \
    "buffer_load_dwordx4 a[104:107], v23, s[36:39], s61 offen offset:0 sc1\n" \
    "buffer_load_dwordx4 a[108:111], v23, s[36:39], s61 offen offset:1024 sc1\n" \
    "s_add_u32 s40, s40, 444416\n" \
    "s_add_u32 s49, s49, 14336\n" \
    "s_add_u32 s51, s51, 25600\n" \
    "s_add_u32 s48, s48, 1\n" \
    "s_branch LX_top%=\n" \
    "LX_last%=:\n" \
    "s_barrier\n" \
    "s_branch LS_end%=\n" \
    "LX_r0%=:\n" \
    "s_add_u32 s52, s52, 1\n" \
    "s_cmp_ge_u32 s52, 2097152\n" \
    "s_cbranch_scc1 LS_ab%=\n" \
    "s_max_u32 s59, s59, s94\n" \
    "buffer_load_dwordx4 v[32:35], v28, s[36:39], s49 offen sc1\n" \
    "buffer_load_dwordx4 v[36:39], v29, s[36:39], s49 offen sc1\n" \
    "s_waitcnt vmcnt(2)\n" \
    "v_max3_u32 v68, v60, v61, v62\n" \
    "v_max3_u32 v69, v63, v64, v65\n" \
    "v_max3_u32 v68, v68, v66, v67\n" \
    "v_max_u32 v68, v68, v69\n" \
    "v_cmp_eq_u32 vcc, -1, v68\n" \
    "s_cbranch_vccnz LX_r1%=\n" \
    "ds_write_b128 v0, v[60:63]\n" \
    "ds_write_b128 v1, v[64:67]\n" \
    "v_smfmac_f32_16x16x64_bf16 v[76:79], v[60:63], a[0:7], v8\n" \
    "v_smfmac_f32_16x16x64_bf16 v[80:83], v[60:63], a[56:63], v8\n" \
    "v_smfmac_f32_16x16x64_bf16 v[76:79], v[64:67], a[8:15], v8\n" \
    "v_smfmac_f32_16x16x64_bf16 v[80:83], v[64:67], a[64:71], v8\n" \
    "s_waitcnt lgkmcnt(0)\n" \
    "s_barrier\n" \
    "ds_read_b128 v[40:43], v2\n" \
    "ds_read_b128 v[44:47], v3\n" \
    "ds_read_b128 v[48:51], v4\n" \
    "ds_read_b128 v[52:55], v5\n" \
    "ds_read_b128 v[56:59], v6\n" \
    "s_branch LX_c%=\n" \
    "LX_r1%=:\n" \
    "s_add_u32 s52, s52, 1\n" \
    "s_cmp_ge_u32 s52, 2097152\n" \
    "s_cbranch_scc1 LS_ab%=\n" \
    "s_max_u32 s59, s59, 0\n" \
    "s_add_u32 s61, s49, 7168\n" \
    "buffer_load_dwordx4 v[60:63], v28, s[36:39], s61 offen sc1\n" \
    "buffer_load_dwordx4 v[64:67], v29, s[36:39], s61 offen sc1\n" \
    "s_branch LX_p0%=\n" \
    "LS_ab%=:\n" \
    "v_mov_b32 v68, 1\n" \
    "v_mov_b32 v69, s53\n" \
    "buffer_store_dword v68, v69, s[36:39], 0 offen sc1\n" \
    "LS_end%=:\n" \
    "s_waitcnt vmcnt(0) lgkmcnt(0)\n"
#define ASM_V \
    "s_mov_b32 s48, 0\n" \
    "s_mov_b32 s52, 0\n" \
    "s_mov_b32 s58, 0\n" \
    "s_mov_b32 s88, 0x11111111\n" \
    "s_mov_b32 s89, 0x11111111\n" \
    "s_add_u32 s61, s44, 0\n" \
    "buffer_load_dwordx4 a[0:3], v13, s[36:39], s61 offen offset:0\n" \
    "buffer_load_dwordx4 a[4:7], v13, s[36:39], s61 offen offset:1024\n" \
    "s_add_u32 s61, s44, 2048\n" \
    "buffer_load_dwordx4 a[8:11], v13, s[36:39], s61 offen offset:0\n" \
    "buffer_load_dwordx4 a[12:15], v13, s[36:39], s61 offen offset:1024\n" \
    "s_add_u32 s61, s44, 4096\n" \
    "buffer_load_dwordx4 a[16:19], v13, s[36:39], s61 offen offset:0\n" \
    "buffer_load_dwordx4 a[20:23], v13, s[36:39], s61 offen offset:1024\n" \
    "s_add_u32 s61, s44, 6144\n" \
    "buffer_load_dwordx4 a[24:27], v13, s[36:39], s61 offen offset:0\n" \
    "buffer_load_dwordx4 a[28:31], v13, s[36:39], s61 offen offset:1024\n" \
    "s_add_u32 s61, s44, 8192\n" \
    "buffer_load_dwordx4 a[32:35], v13, s[36:39], s61 offen offset:0\n" \
    "buffer_load_dwordx4 a[36:39], v13, s[36:39], s61 offen offset:1024\n" \
    "s_add_u32 s61, s44, 10240\n" \
    "buffer_load_dwordx4 a[40:43], v13, s[36:39], s61 offen offset:0\n" \
    "buffer_load_dwordx4 a[44:47], v13, s[36:39], s61 offen offset:1024\n" \
    "s_add_u32 s61, s44, 12288\n" \
    "buffer_load_dwordx4 a[48:51], v13, s[36:39], s61 offen offset:0\n" \
    "buffer_load_dwordx4 a[52:55], v13, s[36:39], s61 offen offset:1024\n" \
    "s_add_u32 s61, s44, 802816\n" \
    "buffer_load_dwordx4 a[56:59], v13, s[36:39], s61 offen offset:0\n" \
    "buffer_load_dwordx4 a[60:63], v13, s[36:39], s61 offen offset:1024\n" \
    "s_add_u32 s61, s44, 804864\n" \
    "buffer_load_dwordx4 a[64:67], v13, s[36:39], s61 offen offset:0\n" \
    "buffer_load_dwordx4 a[68:71], v13, s[36:39], s61 offen offset:1024\n" \
    "s_add_u32 s61, s44, 806912\n" \
    "buffer_load_dwordx4 a[72:75], v13, s[36:39], s61 offen offset:0\n" \
    "buffer_load_dwordx4 a[76:79], v13, s[36:39], s61 offen offset:1024\n" \
    "s_add_u32 s61, s44, 808960\n" \
    "buffer_load_dwordx4 a[80:83], v13, s[36:39], s61 offen offset:0\n" \
    "buffer_load_dwordx4 a[84:87], v13, s[36:39], s61 offen offset:1024\n" \
    "s_add_u32 s61, s44, 811008\n" \
    "buffer_load_dwordx4 a[88:91], v13, s[36:39], s61 offen offset:0\n" \
    "buffer_load_dwordx4 a[92:95], v13, s[36:39], s61 offen offset:1024\n" \
    "s_add_u32 s61, s44, 813056\n" \
    "buffer_load_dwordx4 a[96:99], v13, s[36:39], s61 offen offset:0\n" \
    "buffer_load_dwordx4 a[100:103], v13, s[36:39], s61 offen offset:1024\n" \
    "s_add_u32 s61, s44, 815104\n" \
    "buffer_load_dwordx4 a[104:107], v13, s[36:39], s61 offen offset:0\n" \
    "buffer_load_dwordx4 a[108:111], v13, s[36:39], s61 offen offset:1024\n" \
    "s_add_u32 s61, s44, 401408\n" \
    "buffer_load_dwordx4 a[112:115], v13, s[36:39], s61 offen offset:0\n" \
    "buffer_load_dwordx4 a[116:119], v13, s[36:39], s61 offen offset:1024\n" \
    "s_add_u32 s61, s44, 403456\n" \
    "buffer_load_dwordx4 a[120:123], v13, s[36:39], s61 offen offset:0\n" \
    "buffer_load_dwordx4 a[124:127], v13, s[36:39], s61 offen offset:1024\n" \
    "s_add_u32 s61, s44, 405504\n" \
    "buffer_load_dwordx4 a[128:131], v13, s[36:39], s61 offen offset:0\n" \
    "buffer_load_dwordx4 a[132:135], v13, s[36:39], s61 offen offset:1024\n" \
    "s_add_u32 s61, s44, 407552\n" \
    "buffer_load_dwordx4 a[136:139], v13, s[36:39], s61 offen offset:0\n" \
    "buffer_load_dwordx4 a[140:143], v13, s[36:39], s61 offen offset:1024\n" \
    "s_add_u32 s61, s44, 409600\n" \
    "buffer_load_dwordx4 a[144:147], v13, s[36:39], s61 offen offset:0\n" \
    "buffer_load_dwordx4 a[148:151], v13, s[36:39], s61 offen offset:1024\n" \
    "s_add_u32 s61, s44, 411648\n" \
    "buffer_load_dwordx4 a[152:155], v13, s[36:39], s61 offen offset:0\n" \
    "buffer_load_dwordx4 a[156:159], v13, s[36:39], s61 offen offset:1024\n" \
    "s_add_u32 s61, s44, 413696\n" \
    "buffer_load_dwordx4 a[160:163], v13, s[36:39], s61 offen offset:0\n" \
    "buffer_load_dwordx4 a[164:167], v13, s[36:39], s61 offen offset:1024\n" \
    "s_add_u32 s61, s44, 1204224\n" \
    "buffer_load_dwordx4 a[168:171], v13, s[36:39], s61 offen offset:0\n" \
    "buffer_load_dwordx4 a[172:175], v13, s[36:39], s61 offen offset:1024\n" \
    "s_add_u32 s61, s44, 1206272\n" \
    "buffer_load_dwordx4 a[176:179], v13, s[36:39], s61 offen offset:0\n" \
    "buffer_load_dwordx4 a[180:183], v13, s[36:39], s61 offen offset:1024\n" \
    "s_add_u32 s61, s44, 1208320\n" \
    "buffer_load_dwordx4 a[184:187], v13, s[36:39], s61 offen offset:0\n" \
    "buffer_load_dwordx4 a[188:191], v13, s[36:39], s61 offen offset:1024\n" \
    "s_add_u32 s61, s44, 1210368\n" \
    "buffer_load_dwordx4 a[192:195], v13, s[36:39], s61 offen offset:0\n" \
    "buffer_load_dwordx4 a[196:199], v13, s[36:39], s61 offen offset:1024\n" \
    "s_add_u32 s61, s44, 1212416\n" \
    "buffer_load_dwordx4 a[200:203], v13, s[36:39], s61 offen offset:0\n" \
    "buffer_load_dwordx4 a[204:207], v13, s[36:39], s61 offen offset:1024\n" \
    "s_add_u32 s61, s44, 1214464\n" \
    "buffer_load_dwordx4 a[208:211], v13, s[36:39], s61 offen offset:0\n" \
    "buffer_load_dwordx4 a[212:215], v13, s[36:39], s61 offen offset:1024\n" \
    "s_add_u32 s61, s44, 1216512\n" \
    "buffer_load_dwordx4 a[216:219], v13, s[36:39], s61 offen offset:0\n" \
    "buffer_load_dwordx4 a[220:223], v13, s[36:39], s61 offen offset:1024\n" \
    "s_waitcnt vmcnt(0)\n" \
    "s_memtime s[56:57]\n" \
    "s_waitcnt lgkmcnt(0)\n" \
    "s_sub_u32 s56, s56, s92\n" \
    "LV_top%=:\n" \
    "v_mov_b64 v[48:49], 0\n" \
    "v_mov_b64 v[50:51], 0\n" \
    "v_mov_b64 v[52:53], 0\n" \
    "v_mov_b64 v[54:55], 0\n" \
    "v_mov_b64 v[56:57], 0\n" \
    "v_mov_b64 v[58:59], 0\n" \
    "v_mov_b64 v[60:61], 0\n" \
    "v_mov_b64 v[62:63], 0\n" \
    "v_mov_b64 v[140:141], 0\n" \
    "v_mov_b64 v[142:143], 0\n" \
    "s_mov_b32 s59, 0\n" \
    "LV_w%=:\n" \
    "s_memtime s[54:55]\n" \
    "s_waitcnt lgkmcnt(0)\n" \
    "s_sub_u32 s57, s54, s56\n" \
    "s_cmp_lt_i32 s57, s92\n" \
    "s_cbranch_scc1 LV_w%=\n" \
    "buffer_load_dword v88, v15, s[36:39], s45 offen sc1\n" \
    "buffer_load_dwordx4 v[20:23], v16, s[36:39], s49 offen sc1\n" \
    "buffer_load_dwordx4 v[24:27], v17, s[36:39], s49 offen sc1\n" \
    "LV_p%=:\n" \
    "s_waitcnt vmcnt(0)\n" \
    "v_cmp_eq_u32 s[62:63], 0, v88\n" \
    "v_max3_u32 v89, v20, v21, v22\n" \
    "v_max3_u32 v90, v23, v24, v25\n" \
    "v_max3_u32 v89, v89, v26, v27\n" \
    "v_max_u32 v89, v89, v90\n" \
    "v_cmp_eq_u32 vcc, -1, v89\n" \
    "s_or_b64 vcc, vcc, s[62:63]\n" \
    "s_cbranch_vccz LV_m%=\n" \
    "s_add_u32 s52, s52, 1\n" \
    "s_cmp_ge_u32 s52, 2097152\n" \
    "s_cbranch_scc1 LV_ab%=\n" \
    "s_mov_b32 s59, s94\n" \
    "buffer_load_dword v88, v15, s[36:39], s45 offen sc1\n" \
    "buffer_load_dwordx4 v[20:23], v16, s[36:39], s49 offen sc1\n" \
    "buffer_load_dwordx4 v[24:27], v17, s[36:39], s49 offen sc1\n" \
    "s_branch LV_p%=\n" \
    "LV_m%=:\n" \
    "s_memtime s[56:57]\n" \
    "ds_write_b128 v0, v[20:23]\n" \
    "ds_write_b128 v1, v[24:27]\n" \
    "s_add_u32 s61, s47, 4096\n" \
    "buffer_load_dwordx4 v[96:99], v13, s[36:39], s47 offen offset:0 sc1\n" \
    "buffer_load_dwordx4 v[100:103], v13, s[36:39], s47 offen offset:2048 sc1\n" \
    "buffer_load_dwordx4 v[104:107], v13, s[36:39], s61 offen offset:0 sc1\n" \
    "buffer_load_dwordx4 v[108:111], v13, s[36:39], s61 offen offset:2048 sc1\n" \
    "s_waitcnt lgkmcnt(0)\n" \
    "s_barrier\n" \
    "s_max_u32 s59, s59, s58\n" \
    "s_mov_b32 s58, 0\n" \
    "s_add_u32 s56, s56, s59\n" \
    "ds_read_b128 v[20:23], v2 offset:0\n" \
    "ds_read_b128 v[24:27], v2 offset:1024\n" \
    "ds_read_b128 v[28:31], v2 offset:2048\n" \
    "ds_read_b128 v[32:35], v2 offset:3072\n" \
    "ds_read_b128 v[36:39], v2 offset:4096\n" \
    "ds_read_b128 v[40:43], v2 offset:5120\n" \
    "ds_read_b128 v[44:47], v2 offset:6144\n" \
    "s_lshl_b32 s61, s48, 8\n" \
    "buffer_store_dword v4, v3, s[36:39], s61 offen sc1\n" \
    "s_waitcnt lgkmcnt(6)\n" \
    "v_smfmac_f32_16x16x64_bf16 v[48:51], v[20:23], a[0:7], v8\n" \
    "v_smfmac_f32_16x16x64_bf16 v[52:55], v[20:23], a[56:63], v8\n" \
    "v_smfmac_f32_16x16x64_bf16 v[56:59], v[20:23], a[112:119], v8\n" \
    "v_smfmac_f32_16x16x64_bf16 v[60:63], v[20:23], a[168:175], v8\n" \
    "s_waitcnt lgkmcnt(5)\n" \
    "v_smfmac_f32_16x16x64_bf16 v[48:51], v[24:27], a[8:15], v8\n" \
    "v_smfmac_f32_16x16x64_bf16 v[52:55], v[24:27], a[64:71], v8\n" \
    "v_smfmac_f32_16x16x64_bf16 v[56:59], v[24:27], a[120:127], v8\n" \
    "v_smfmac_f32_16x16x64_bf16 v[60:63], v[24:27], a[176:183], v8\n" \
    "s_waitcnt lgkmcnt(4)\n" \
    "v_smfmac_f32_16x16x64_bf16 v[48:51], v[28:31], a[16:23], v8\n" \
    "v_smfmac_f32_16x16x64_bf16 v[52:55], v[28:31], a[72:79], v8\n" \
    "v_smfmac_f32_16x16x64_bf16 v[56:59], v[28:31], a[128:135], v8\n" \
    "v_smfmac_f32_16x16x64_bf16 v[60:63], v[28:31], a[184:191], v8\n" \
    "s_waitcnt lgkmcnt(3)\n" \
    "v_smfmac_f32_16x16x64_bf16 v[48:51], v[32:35], a[24:31], v8\n" \
    "v_smfmac_f32_16x16x64_bf16 v[52:55], v[32:35], a[80:87], v8\n" \
    "v_smfmac_f32_16x16x64_bf16 v[56:59], v[32:35], a[136:143], v8\n" \
    "v_smfmac_f32_16x16x64_bf16 v[60:63], v[32:35], a[192:199], v8\n" \
    "s_waitcnt lgkmcnt(2)\n" \
    "v_smfmac_f32_16x16x64_bf16 v[48:51], v[36:39], a[32:39], v8\n" \
    "v_smfmac_f32_16x16x64_bf16 v[52:55], v[36:39], a[88:95], v8\n" \
    "v_smfmac_f32_16x16x64_bf16 v[56:59], v[36:39], a[144:151], v8\n" \
    "v_smfmac_f32_16x16x64_bf16 v[60:63], v[36:39], a[200:207], v8\n" \
    "s_waitcnt lgkmcnt(1)\n" \
    "v_smfmac_f32_16x16x64_bf16 v[48:51], v[40:43], a[40:47], v8\n" \
    "v_smfmac_f32_16x16x64_bf16 v[52:55], v[40:43], a[96:103], v8\n" \
    "v_smfmac_f32_16x16x64_bf16 v[56:59], v[40:43], a[152:159], v8\n" \
    "v_smfmac_f32_16x16x64_bf16 v[60:63], v[40:43], a[208:215], v8\n" \
    "s_waitcnt lgkmcnt(0)\n" \
    "v_smfmac_f32_16x16x64_bf16 v[48:51], v[44:47], a[48:55], v8\n" \
    "v_smfmac_f32_16x16x64_bf16 v[52:55], v[44:47], a[104:111], v8\n" \
    "v_smfmac_f32_16x16x64_bf16 v[56:59], v[44:47], a[160:167], v8\n" \
    "v_smfmac_f32_16x16x64_bf16 v[60:63], v[44:47], a[216:223], v8\n" \
    "s_nop 0\n" \
    "v_permlane32_swap_b32 v48, v52\n" \
    "v_permlane32_swap_b32 v49, v53\n" \
    "v_permlane32_swap_b32 v50, v54\n" \
    "v_permlane32_swap_b32 v51, v55\n" \
    "v_pk_add_f32 v[48:49], v[48:49], v[52:53]\n" \
    "v_pk_add_f32 v[50:51], v[50:51], v[54:55]\n" \
    "v_cvt_pk_bf16_f32 v132, v48, v49\n" \
    "v_cvt_pk_bf16_f32 v134, v50, v51\n" \
    "v_permlane32_swap_b32 v56, v60\n" \
    "v_permlane32_swap_b32 v57, v61\n" \
    "v_permlane32_swap_b32 v58, v62\n" \
    "v_permlane32_swap_b32 v59, v63\n" \
    "v_pk_add_f32 v[56:57], v[56:57], v[60:61]\n" \
    "v_pk_add_f32 v[58:59], v[58:59], v[62:63]\n" \
    "v_cvt_pk_bf16_f32 v133, v56, v57\n" \
    "v_cvt_pk_bf16_f32 v135, v58, v59\n" \
    "buffer_store_dwordx4 v[132:135], v13, s[36:39], s50 offen sc1\n" \
    "s_add_u32 s50, s50, 114688\n" \
    "s_cmp_eq_u32 s48, 63\n" \
    "s_cbranch_scc1 LV_last%=\n" \
    "LV_z%=:\n" \
    "s_memtime s[54:55]\n" \
    "s_waitcnt lgkmcnt(0)\n" \
    "s_sub_u32 s57, s54, s56\n" \
    "s_cmp_lt_i32 s57, s95\n" \
    "s_cbranch_scc1 LV_z%=\n" \
    "buffer_load_dwordx4 v[112:115], v9, s[36:39], s51 offen offset:0 sc1\n" \
    "buffer_load_dwordx4 v[116:119], v9, s[36:39], s51 offen offset:512 sc1\n" \
    "buffer_load_dwordx4 v[120:123], v10, s[36:39], s51 offen offset:0 sc1\n" \
    "buffer_load_dwordx4 v[124:127], v10, s[36:39], s51 offen offset:512 sc1\n" \
    "buffer_load_dwordx2 v[128:129], v18, s[36:39], s51 offen sc1\n" \
    "LV_x%=:\n" \
    "s_memtime s[54:55]\n" \
    "s_waitcnt lgkmcnt(0)\n" \
    "s_sub_u32 s57, s54, s56\n" \
    "s_cmp_lt_i32 s57, s93\n" \
    "s_cbranch_scc1 LV_x%=\n" \
    "buffer_load_dwordx4 v[72:75], v19, s[36:39], s51 offen sc1\n" \
    "buffer_load_dwordx4 v[76:79], v19, s[36:39], s51 offen offset:256 sc1\n" \
    "s_mov_b32 s59, 1\n" \
    "s_waitcnt vmcnt(2)\n" \
    "LV_y%=:\n" \
    "v_mfma_f32_16x16x32_bf16 v[48:51], v[112:115], v[96:99], 0\n" \
    "v_or3_b32 v88, v112, v113, v114\n" \
    "v_or3_b32 v89, v115, v116, v117\n" \
    "v_or3_b32 v88, v88, v118, v119\n" \
    "v_mfma_f32_16x16x32_bf16 v[52:55], v[116:119], v[100:103], 0\n" \
    "v_or3_b32 v89, v89, v120, v121\n" \
    "v_or3_b32 v88, v88, v122, v123\n" \
    "v_or3_b32 v89, v89, v124, v125\n" \
    "v_mfma_f32_16x16x32_bf16 v[48:51], v[120:123], v[104:107], v[48:51]\n" \
    "v_or3_b32 v88, v88, v126, v127\n" \
    "v_and_b32 v90, 0x80000000, v129\n" \
    "v_or3_b32 v88, v88, v89, v90\n" \
    "v_mfma_f32_16x16x32_bf16 v[52:55], v[124:127], v[108:111], v[52:55]\n" \
    "v_and_b32 v88, 0x80008000, v88\n" \
    "v_cmp_ne_u32 vcc, 0, v88\n" \
    "s_nop 1\n" \
    "v_cvt_pk_bf16_f32 v136, v48, v49\n" \
    "v_cvt_pk_bf16_f32 v138, v50, v51\n" \
    "s_nop 1\n" \
    "v_cvt_pk_bf16_f32 v137, v52, v53\n" \
    "v_cvt_pk_bf16_f32 v139, v54, v55\n" \
    "s_cbranch_vccnz LV_xr%=\n" \
    "s_add_u32 s61, s51, 8192\n" \
    "buffer_load_dwordx4 v[80:83], v19, s[36:39], s61 offen sc1\n" \
    "buffer_load_dwordx4 v[84:87], v19, s[36:39], s61 offen offset:256 sc1\n" \
    "s_waitcnt vmcnt(2)\n" \
    "LV_q%=:\n" \
    "v_pk_add_f32 v[48:49], v[72:73], v[76:77]\n" \
    "v_pk_add_f32 v[50:51], v[74:75], v[78:79]\n" \
    "v_max3_f32 v52, v48, v49, v128\n" \
    "v_max3_f32 v52, v52, v50, v51\n" \
    "v_mov_b32 v53, v52\n" \
    "v_max3_u32 v88, v72, v73, v74\n" \
    "v_max3_u32 v89, v75, v76, v77\n" \
    "v_permlane32_swap_b32 v52, v53\n" \
    "v_max_f32 v52, v52, v53\n" \
    "v_pk_add_f32 v[48:49], v[48:49], v[52:53] op_sel_hi:[1,0] neg_lo:[0,1] neg_hi:[0,1]\n" \
    "v_pk_add_f32 v[50:51], v[50:51], v[52:53] op_sel_hi:[1,0] neg_lo:[0,1] neg_hi:[0,1]\n" \
    "v_sub_f32 v60, v128, v52\n" \
    "v_exp_f32 v56, v48\n" \
    "v_exp_f32 v57, v49\n" \
    "v_exp_f32 v58, v50\n" \
    "v_exp_f32 v59, v51\n" \
    "v_exp_f32 v60, v60\n" \
    "v_pk_add_f32 v[52:53], v[56:57], v[58:59]\n" \
    "v_add_f32 v52, v52, v53\n" \
    "v_mov_b32 v53, v52\n" \
    "v_max3_u32 v88, v88, v78, v79\n" \
    "v_max_u32 v88, v88, v89\n" \
    "v_permlane32_swap_b32 v52, v53\n" \
    "v_add_f32 v52, v52, v53\n" \
    "v_fmac_f32 v52, v60, v129\n" \
    "v_rcp_f32 v52, v52\n" \
    "v_cmp_eq_u32 vcc, -1, v88\n" \
    "v_mul_f32 v60, v60, v52\n" \
    "v_pk_mul_f32 v[56:57], v[56:57], v[52:53] op_sel_hi:[1,0]\n" \
    "v_pk_mul_f32 v[58:59], v[58:59], v[52:53] op_sel_hi:[1,0]\n" \
    "v_cvt_pk_bf16_f32 v52, v60, v60\n" \
    "v_cvt_pk_bf16_f32 v20, v56, v57\n" \
    "v_cvt_pk_bf16_f32 v21, v58, v59\n" \
    "v_and_b32 v22, v5, v52\n" \
    "v_and_b32 v23, v6, v52\n" \
    "s_cbranch_vccnz LV_r%=\n" \
    "v_permlane32_swap_b32 v20, v22\n" \
    "v_permlane32_swap_b32 v21, v23\n" \
    "s_nop 1\n" \
    "v_smfmac_f32_16x16x64_bf16 v[140:143], v[20:23], v[132:139], v8\n" \
    "s_add_u32 s49, s49, 14336\n" \
    "s_nop 6\n" \
    "ds_write2_b32 v11, v140, v141 offset0:0 offset1:16\n" \
    "ds_write2_b32 v11, v142, v143 offset0:32 offset1:48\n" \
    "s_waitcnt lgkmcnt(0)\n" \
    "s_barrier\n" \
    "s_cmp_lg_u32 s46, 0\n" \
    "s_cbranch_scc1 LV_idle%=\n" \
    "ds_read_b64 v[48:49], v12 offset:0\n" \
    "ds_read_b64 v[50:51], v12 offset:640\n" \
    "ds_read_b64 v[52:53], v12 offset:1280\n" \
    "ds_read_b64 v[54:55], v12 offset:1920\n" \
    "ds_read_b64 v[56:57], v12 offset:2560\n" \
    "ds_read_b64 v[58:59], v12 offset:3200\n" \
    "ds_read_b64 v[60:61], v12 offset:3840\n" \
    "ds_read_b64 v[62:63], v12 offset:4480\n" \
    "s_waitcnt lgkmcnt(6)\n" \
    "v_pk_add_f32 v[48:49], v[48:49], v[50:51]\n" \
    "s_waitcnt lgkmcnt(4)\n" \
    "v_pk_add_f32 v[52:53], v[52:53], v[54:55]\n" \
    "v_pk_add_f32 v[48:49], v[48:49], v[52:53]\n" \
    "s_waitcnt lgkmcnt(2)\n" \
    "v_pk_add_f32 v[56:57], v[56:57], v[58:59]\n" \
    "v_pk_add_f32 v[48:49], v[48:49], v[56:57]\n" \
    "s_waitcnt lgkmcnt(0)\n" \
    "v_pk_add_f32 v[60:61], v[60:61], v[62:63]\n" \
    "v_pk_add_f32 v[48:49], v[48:49], v[60:61]\n" \
    "v_cvt_pk_bf16_f32 v48, v48, v49\n" \
    "s_nop 1\n" \
    "v_mov_b32_dpp v49, v48 quad_perm:[1,1,1,1] row_mask:0xf bank_mask:0xf\n" \
    "v_mov_b32_dpp v50, v48 quad_perm:[2,2,2,2] row_mask:0xf bank_mask:0xf\n" \
    "v_mov_b32_dpp v51, v48 quad_perm:[3,3,3,3] row_mask:0xf bank_mask:0xf\n" \
    "s_mov_b64 exec, s[88:89]\n" \
    "buffer_store_dwordx4 v[48:51], v14, s[36:39], s49 offen sc1\n" \
    "s_add_u32 s61, s49, 7168\n" \
    "buffer_store_dwordx4 v[48:51], v14, s[36:39], s61 offen sc1\n" \
    "s_mov_b64 exec, -1\n" \
    "s_nop 0\n" \
    "s_branch LV_j%=\n" \
    "LV_idle%=:\n" \
    "s_sleep 2\n" \
    "s_nop 7\n" \
    "LV_j%=:\n" \
    "s_memtime s[56:57]\n" \
    "s_waitcnt lgkmcnt(0)\n" \
    "s_add_u32 s51, s51, 25600\n" \
    "s_add_u32 s47, s47, 458752\n" \
    "s_add_u32 s48, s48, 1\n" \
    "s_and_b32 s61, s48, 1\n" \
    "s_cmp_eq_u32 s61, 0\n" \
    "s_cselect_b32 s61, 256, 0\n" \
    "s_add_u32 s45, s45, s61\n" \
    "s_branch LV_top%=\n" \
    "LV_last%=:\n" \
    "s_waitcnt vmcnt(0)\n" \
    "s_barrier\n" \
    "s_add_u32 s49, s49, 14336\n" \
    "s_cmp_lg_u32 s46, 0\n" \
    "s_cbranch_scc1 LV_end%=\n" \
    "s_mov_b64 exec, 1\n" \
    "v_mov_b32 v48, 1\n" \
    "buffer_store_dword v48, v14, s[36:39], s49 offen sc1\n" \
    "s_mov_b64 exec, -1\n" \
    "s_branch LV_end%=\n" \
    "LV_xr%=:\n" \
    "s_mov_b32 s58, 0\n" \
    "s_add_u32 s52, s52, 1\n" \
    "s_cmp_ge_u32 s52, 2097152\n" \
    "s_cbranch_scc1 LV_ab%=\n" \
    "buffer_load_dwordx4 v[112:115], v9, s[36:39], s51 offen offset:0 sc1\n" \
    "buffer_load_dwordx4 v[116:119], v9, s[36:39], s51 offen offset:512 sc1\n" \
    "buffer_load_dwordx4 v[120:123], v10, s[36:39], s51 offen offset:0 sc1\n" \
    "buffer_load_dwordx4 v[124:127], v10, s[36:39], s51 offen offset:512 sc1\n" \
    "buffer_load_dwordx2 v[128:129], v18, s[36:39], s51 offen sc1\n" \
    "s_waitcnt vmcnt(0)\n" \
    "s_branch LV_y%=\n" \
    "LV_r%=:\n" \
    "s_mov_b32 s58, 0\n" \
    "s_add_u32 s52, s52, 1\n" \
    "s_cmp_ge_u32 s52, 2097152\n" \
    "s_cbranch_scc1 LV_ab%=\n" \
    "s_waitcnt vmcnt(0)\n" \
    "v_mov_b64 v[72:73], v[80:81]\n" \
    "v_mov_b64 v[74:75], v[82:83]\n" \
    "v_mov_b64 v[76:77], v[84:85]\n" \
    "v_mov_b64 v[78:79], v[86:87]\n" \
    "s_mov_b32 s59, 1\n" \
    "s_add_u32 s61, s51, 8192\n" \
    "buffer_load_dwordx4 v[80:83], v19, s[36:39], s61 offen sc1\n" \
    "buffer_load_dwordx4 v[84:87], v19, s[36:39], s61 offen offset:256 sc1\n" \
    "s_branch LV_q%=\n" \
    "LV_ab%=:\n" \
    "v_mov_b32 v48, 1\n" \
    "v_mov_b32 v49, s53\n" \
    "buffer_store_dword v48, v49, s[36:39], 0 offen sc1\n" \
    "LV_end%=:\n" \
    "s_waitcnt vmcnt(0) lgkmcnt(0)\n"
#define CLOB_S \
    "v32", "v33", "v34", "v35", "v36", "v37", "v38", "v39", "v40", "v41", "v42", "v43", "v44", "v45", "v46", "v47", "v48", "v49", "v50", "v51", "v52", "v53", "v54", "v55", \
    "v56", "v57", "v58", "v59", "v60", "v61", "v62", "v63", "v64", "v65", "v66", "v67", "v68", "v69", "v70", "v71", "v72", "v73", "v74", "v75", "v76", "v77", "v78", "v79", \
    "v80", "v81", "v82", "v83", "v84", "v85", "v86", "v87", "v88", "v89", "v90", "v91", "v92", "v93", "v94", "v95", "v96", "v97", "v98", "v99", "v100", "v101", "v102", "v103", \
    "v104", "v105", "v106", "v107", "v108", "v109", "v110", "v111", "v112", "v113", "v114", "v115", "v116", "v117", "v118", "v119", "v120", "v121", "v122", "v123", "v124", "v125", "v126", "v127", \
    "v128", "v129", "v130", "v131", "v132", "v133", "v134", "v135", "v136", "v137", "v138", "v139", "v140", "v141", "v142", "v143", "v144", "v145", "a0", "a1", "a2", "a3", "a4", "a5", \
    "a6", "a7", "a8", "a9", "a10", "a11", "a12", "a13", "a14", "a15", "a16", "a17", "a18", "a19", "a20", "a21", "a22", "a23", "a24", "a25", "a26", "a27", "a28", "a29", \
    "a30", "a31", "a32", "a33", "a34", "a35", "a36", "a37", "a38", "a39", "a40", "a41", "a42", "a43", "a44", "a45", "a46", "a47", "a48", "a49", "a50", "a51", "a52", "a53", \
    "a54", "a55", "a56", "a57", "a58", "a59", "a60", "a61", "a62", "a63", "a64", "a65", "a66", "a67", "a68", "a69", "a70", "a71", "a72", "a73", "a74", "a75", "a76", "a77", \
    "a78", "a79", "a80", "a81", "a82", "a83", "a84", "a85", "a86", "a87", "a88", "a89", "a90", "a91", "a92", "a93", "a94", "a95", "a96", "a97", "a98", "a99", "a100", "a101", \
    "a102", "a103", "a104", "a105", "a106", "a107", "a108", "a109", "a110", "a111", "a112", "a113", "a114", "a115", "a116", "a117", "a118", "a119", "a120", "a121", "a122", "a123", "a124", "a125", \
    "a126", "a127", "a128", "a129", "a130", "a131", "a132", "a133", "a134", "a135", "a136", "a137", "a138", "a139", "a140", "a141", "a142", "a143", "a144", "a145", "a146", "a147", "a148", "a149", \
    "a150", "a151", "a152", "a153", "a154", "a155", "a156", "a157", "a158", "a159", "a160", "a161", "a162", "a163", "a164", "a165", "a166", "a167", "a168", "a169", "a170", "a171", "a172", "a173", \
    "a174", "a175", "a176", "a177", "a178", "a179", "a180", "a181", "a182", "a183", "a184", "a185", "a186", "a187", "a188", "a189", "a190", "a191", "a192", "a193", "a194", "a195", "a196", "a197", \
    "a198", "a199", "a200", "a201", "a202", "a203", "a204", "a205", "a206", "a207", "a208", "a209", "a210", "a211", "a212", "a213", "a214", "a215", "a216", "a217", "a218", "a219", "a220", "a221", \
    "a222", "a223", "a224", "a225", "a226", "a227", "a228", "a229", "a230", "a231", "a232", "a233", "a234", "a235", "a236", "a237", "a238", "a239", "a240", "a241", "a242", "a243", "a244", "a245", \
    "a246", "a247", "a248", "a249", "a250", "a251", "a252", "a253", "a254", "a255", "s52", "s54", "s55", "s56", "s57", "s58", "s59", "s61", "s62", "s63", "s64", "s65", "s66", "s67", \
    "s68", "s69", "s70", "s71", "s72", "s73", "s74", "s75", "s76", "s77", "s78", "s79", "s80", "s81", "s82", "s83", "s84", "s85", "s86", "s87", "s88", "s89", "s90", "s91", \
    "vcc", "scc", "memory"
#define CLOB_V \
    "v20", "v21", "v22", "v23", "v24", "v25", "v26", "v27", "v28", "v29", "v30", "v31", "v32", "v33", "v34", "v35", "v36", "v37", "v38", "v39", "v40", "v41", "v42", "v43", \
    "v44", "v45", "v46", "v47", "v48", "v49", "v50", "v51", "v52", "v53", "v54", "v55", "v56", "v57", "v58", "v59", "v60", "v61", "v62", "v63", "v64", "v65", "v66", "v67", \
    "v68", "v69", "v70", "v71", "v72", "v73", "v74", "v75", "v76", "v77", "v78", "v79", "v80", "v81", "v82", "v83", "v84", "v85", "v86", "v87", "v88", "v89", "v90", "v91", \
    "v92", "v93", "v94", "v95", "v96", "v97", "v98", "v99", "v100", "v101", "v102", "v103", "v104", "v105", "v106", "v107", "v108", "v109", "v110", "v111", "v112", "v113", "v114", "v115", \
    "v116", "v117", "v118", "v119", "v120", "v121", "v122", "v123", "v124", "v125", "v126", "v127", "v128", "v129", "v130", "v131", "v132", "v133", "v134", "v135", "v136", "v137", "v138", "v139", \
    "v140", "v141", "v142", "v143", "v144", "v145", "a0", "a1", "a2", "a3", "a4", "a5", "a6", "a7", "a8", "a9", "a10", "a11", "a12", "a13", "a14", "a15", "a16", "a17", \
    "a18", "a19", "a20", "a21", "a22", "a23", "a24", "a25", "a26", "a27", "a28", "a29", "a30", "a31", "a32", "a33", "a34", "a35", "a36", "a37", "a38", "a39", "a40", "a41", \
    "a42", "a43", "a44", "a45", "a46", "a47", "a48", "a49", "a50", "a51", "a52", "a53", "a54", "a55", "a56", "a57", "a58", "a59", "a60", "a61", "a62", "a63", "a64", "a65", \
    "a66", "a67", "a68", "a69", "a70", "a71", "a72", "a73", "a74", "a75", "a76", "a77", "a78", "a79", "a80", "a81", "a82", "a83", "a84", "a85", "a86", "a87", "a88", "a89", \
    "a90", "a91", "a92", "a93", "a94", "a95", "a96", "a97", "a98", "a99", "a100", "a101", "a102", "a103", "a104", "a105", "a106", "a107", "a108", "a109", "a110", "a111", "a112", "a113", \
    "a114", "a115", "a116", "a117", "a118", "a119", "a120", "a121", "a122", "a123", "a124", "a125", "a126", "a127", "a128", "a129", "a130", "a131", "a132", "a133", "a134", "a135", "a136", "a137", \
    "a138", "a139", "a140", "a141", "a142", "a143", "a144", "a145", "a146", "a147", "a148", "a149", "a150", "a151", "a152", "a153", "a154", "a155", "a156", "a157", "a158", "a159", "a160", "a161", \
    "a162", "a163", "a164", "a165", "a166", "a167", "a168", "a169", "a170", "a171", "a172", "a173", "a174", "a175", "a176", "a177", "a178", "a179", "a180", "a181", "a182", "a183", "a184", "a185", \
    "a186", "a187", "a188", "a189", "a190", "a191", "a192", "a193", "a194", "a195", "a196", "a197", "a198", "a199", "a200", "a201", "a202", "a203", "a204", "a205", "a206", "a207", "a208", "a209", \
    "a210", "a211", "a212", "a213", "a214", "a215", "a216", "a217", "a218", "a219", "a220", "a221", "a222", "a223", "a224", "a225", "a226", "a227", "a228", "a229", "a230", "a231", "a232", "a233", \
    "a234", "a235", "a236", "a237", "a238", "a239", "a240", "a241", "a242", "a243", "a244", "a245", "a246", "a247", "a248", "a249", "a250", "a251", "a252", "a253", "a254", "a255", "s52", "s54", \
    "s55", "s56", "s57", "s58", "s59", "s61", "s62", "s63", "s64", "s65", "s66", "s67", "s68", "s69", "s70", "s71", "s72", "s73", "s74", "s75", "s76", "s77", "s78", "s79", \
    "s80", "s81", "s82", "s83", "s84", "s85", "s86", "s87", "s88", "s89", "s90", "s91", "vcc", "scc", "memory"

typedef unsigned u16v __attribute__((ext_vector_type(16)));
constexpr unsigned O_TR = 0, WS_RANGE = WS_BYTES;
DEV u4 srd(const void* p, unsigned n) { const unsigned long long q = (unsigned long long)p; return u4{(unsigned)q, (unsigned)(q >> 32) & 0xFFFFu, n, 0x00020000u}; }

DEV void role_S(const Args& a, lptr lds, int h, int hb) {
    const int t = threadIdx.x, w = __builtin_amdgcn_readfirstlane(t >> 6), l = t & 63, g = l >> 4, c = l & 15;
    constexpr int L_RED = 7424;
    const unsigned lb = (unsigned)(size_t)lds;
    const int p0 = 2 * w, u = 4 * hb + w;
    const bool xw = u == 7;
    u16v c0 = {}, c1 = {};
#pragma unroll
    for (int k = 0; k < 7; ++k) c0[k] = lb + (p0 + k) % 7 * 1024 + l * 16;
    c0[8] = (l & 8) ? 0xEEEE : 0x4444;
    c0[12] = lb + (c & 7) * 16 + memoff(64 * u + 8 * g); c0[13] = lb + (c & 7) * 16 + memoff(64 * u + 32 + 8 * g);
    c1[2] = lb + L_RED + w * 512 + g * 128 + (c & 7) * 16;
    c1[3] = lb + L_RED + g * 512 + c * 16;
    c1[4] = xw ? h * 512 + (2 * (g & 1) + (g >> 1)) * 64 + (c >> 2) * 16 : 9216 + (g & 1) * 8192 + (2 * h + hb) * 256 + c * 16;
    c1[5] = xw ? 8192 + h * 64 + g * 16 : u * 1024 + l * 16;
    c1[7] = l * 16;
#pragma unroll
    for (int d = 0; d < 4; ++d) {
        const int j = 2 * d, n0 = ((4 * g + (j & 3)) & 7) + 8 * (j >> 2);
        c1[8 + d] = (c == n0 ? 0x3F80u : 0u) | (c == n0 + 1 ? 0x3F800000u : 0u);
    }
    c1[12] = p0 * 1024 + l * 16; c1[13] = (p0 + 1) % 7 * 1024 + l * 16;
    u4 tl;
#pragma unroll
    for (int n = 0; n < 4; ++n) tl[n] = O_AW + (h * 28 + (u < 4 ? 7 * u + n : u < 6 ? 7 * (2 * u - 8 + (n >> 1)) + 4 + (n & 1) : 7 * n + 6)) * 14336u;
    if (xw) { tl[0] = O_QB + h * 28672u; tl[1] = F_Q + h * 16; c1[14] = (l & 3) * 4; lw16(lds + L_RED + 1536 + (l & 31) * 16, u4{0, 0, 0, 0}); }
    u8v sc = {0u, (unsigned)p0, xw ? 4u : (unsigned)w, F_S + (2 * h + hb) * 4, 0u, O_MEM, O_KM + h * 7168u, O_PB};
    unsigned trc = O_TR + ((h + 32 * hb) * 4 + w) * 2048u;
    asm volatile(ASM_S : "+{s[40:43]}"(tl), "+{s[44:51]}"(sc), "+{s60}"(trc)
                 : "{v[0:15]}"(c0), "{v[16:31]}"(c1), "{s[36:39]}"(srd(a.ws, WS_RANGE)), "{s53}"(F_AB), "{s92}"(TUNE(0, 2300u)), "{s94}"(TUNE(3, 100u)) : CLOB_S);
    __builtin_amdgcn_endpgm();
}

DEV void role_V(const Args& a, lptr lds, int v) {
    const int t = threadIdx.x, w = __builtin_amdgcn_readfirstlane(t >> 6), l = t & 63, g = l >> 4, c = l & 15;
    constexpr int L_RED = 7424;
    const unsigned lb = (unsigned)(size_t)lds;
    const int m = l & 7, hf = (l >> 3) & 1, hd = 4 * w + 2 * (g & 1) + hf, mr = (l >> 2) & 7;
    const unsigned v0 = w * 1024 + l * 16, v1 = (w < 3 ? w + 4 : w) * 1024 + l * 16;
    u16v c0; u4 c1;
    c0[0] = lb + v0; c0[1] = lb + v1; c0[2] = lb + l * 16; c0[3] = (v == 0 && t == 192) ? F_P : 0x7FFFFF00u; c0[4] = 1;
    c0[5] = (m >> 1) == 2 * (l >> 5) ? 0xFFFFu << 16 * (m & 1) : 0u; c0[6] = (m >> 1) == 2 * (l >> 5) + 1 ? 0xFFFFu << 16 * (m & 1) : 0u;
    c0[7] = 0;
    c0[8] = (l & 8) ? 0xEEEE : 0x4444;
    const unsigned px = (4 * w + 2 * hf) * 512 + (((m >> 1) & 1) * 4 + (m >> 2) * 2 + (m & 1)) * 64 + g * 16;
    c0[9] = hf ? 0x7FFFF000u : px; c0[10] = hf ? px : 0x7FFFF000u;
    c0[11] = lb + L_RED + w * 1280 + g * 320 + c * 4;
    c0[12] = lb + L_RED + (mr >> 2) * 320 + (mr & 3) * 64 + (l & 3) * 16 + (l >> 5) * 8;
    c0[13] = l * 16; c0[14] = v * 256 + l * 4; c0[15] = l * 4;
    c1 = u4{v0, v1, 8192u + hd * 64 + ((m >> 2) + 2 * (m & 1)) * 16 + ((m >> 1) & 1) * 8, 9216u + hd * 512 + (l >> 5) * 128 + m * 16};
    u8v sc = {O_CW + (4 * w * 28 + v) * 14336u, F_U, (unsigned)w, O_UB + ((v >> 1) * 16 + 4 * w) * 2048u + (v & 1) * 1024, 0u, O_MEM,
              O_WB + ((v >> 1) * 4 + w) * 2048u + (v & 1) * 1024, O_PB};
    unsigned trc = O_TR + ((v < 16 ? 16 + v : 32 + v) * 4 + w) * 2048u;
    asm volatile(ASM_V : "+{s[44:51]}"(sc), "+{s60}"(trc)
                 : "{v[0:15]}"(c0), "{v[16:19]}"(c1), "{s[36:39]}"(srd(a.ws, WS_RANGE)), "{s53}"(F_AB), "{s92}"(TUNE(1, 500u)), "{s93}"(TUNE(2, 1500u)), "{s94}"(TUNE(3, 100u)),
                   "{s95}"(TUNE(4, 1200u)) : CLOB_V);
    __builtin_amdgcn_endpgm();
}

DEV void role_K(const Args& a, lptr lds, int h, int s0) {
    const int t = threadIdx.x, w = __builtin_amdgcn_readfirstlane(t >> 6), l = t & 63, g = l >> 4, c = l & 15;
    rsrc_t ws = mkrs(a.ws, WS_BYTES);
    const int idx = (l & 8) ? 0xEEEE : 0x4444;
    const int nu = w < 3 ? 2 : 1;
    constexpr int L_M = 16384;
    bool alive = true;
    for (int s = s0; s < 64; s += 6) {
        wait_flags(ws, F_Q + (s >> 1) * 256 + h * 16, 4, alive);
        const f4 Z = {0, 0, 0, 0};
        u8v X[2][2];
        f4 x00 = Z, x01 = Z, x11 = Z;
#pragma unroll
        for (int i = 0; i < 2; ++i) {
            if (i < nu) {
                const int u = w + 4 * i;
                u4 Kk[2][2];
#pragma unroll
                for (int e = 0; e < 2; ++e) {
                    const unsigned so = O_QB + (s * 16 + h) * 28672u + e * 14336 + u * 2048, xo = O_XA + ((2 * s + e) * 14 + 2 * u) * 1024u;
                    Kk[e][0] = ldc16(ws, l * 16, so); Kk[e][1] = ldc16(ws, l * 16 + 1024, so);
                    X[i][e] = cat(ld16(ws, l * 16, xo), ld16(ws, l * 16 + 1024, xo));
                }
                x00 = mfma(Kk[0][0], lo4(X[i][0]), x00); x01 = mfma(Kk[0][0], lo4(X[i][1]), x01); x11 = mfma(Kk[1][0], lo4(X[i][1]), x11);
                x00 = mfma(Kk[0][1], hi4(X[i][0]), x00); x01 = mfma(Kk[0][1], hi4(X[i][1]), x01); x11 = mfma(Kk[1][1], hi4(X[i][1]), x11);
            }
        }
        lwf4(lds + w * 3072 + l * 16, x00); lwf4(lds + w * 3072 + 1024 + l * 16, x01); lwf4(lds + w * 3072 + 2048 + l * 16, x11);
        bar();
        f4 la = Z, lb = Z;
        if (w < 2) {
            lptr rr = lds + w * 1024 + l * 16;
            la = (lrf4(rr) + lrf4(rr + 3072)) + (lrf4(rr + 6144) + lrf4(rr + 9216));
            rr = lds + 2048 + l * 16;
            lb = (lrf4(rr) + lrf4(rr + 3072)) + (lrf4(rr + 6144) + lrf4(rr + 9216));
#pragma unroll
            for (int r = 0; r < 4; ++r) {
                if (w == 0 && 4 * g + r > c) la[r] = -__builtin_inff();
                if (w == 0 || 4 * g + r > c) lb[r] = -__builtin_inff();
            }
        }
        wait_flags(ws, F_P + (s < 62 ? s + 2 : s) * 256, 1, alive);
        u4 A0, A1;
        for (unsigned spins = 0;;) { asm volatile("" ::: "memory");
            A0 = ldc16(ws, w * 1024 + l * 16, O_KM + (s * 16 + h) * 7168u); A1 = ldc16(ws, (w < 3 ? w + 4 : w) * 1024 + l * 16, O_KM + (s * 16 + h) * 7168u);
            if (s < 62 || !alive || __all(umax4(A0, umax4(A1, 0)) != ~0u)) break;
            if (spin_fail(ws, spins)) alive = false;
            __builtin_amdgcn_s_sleep(4);
        }
        f4 m0 = smf(A0, X[0][0], Z, idx), m1 = smf(A0, X[0][1], Z, idx);
        if (nu == 2) { m0 = smf(A1, X[1][0], m0, idx); m1 = smf(A1, X[1][1], m1, idx); }
        lwf4(lds + L_M + w * 1024 + (l >> 5) * 512 + c * 32 + (g & 1) * 16, f4{hsum(m0[0], m1[0]), hsum(m0[1], m1[1]), hsum(m0[2], m1[2]), hsum(m0[3], m1[3])});
        bar();
        if (w < 2) {
            lptr rr = lds + L_M + w * 512 + c * 32 + g * 8;
            const f2 lm = (lrf2(rr) + lrf2(rr + 1024)) + (lrf2(rr + 2048) + lrf2(rr + 3072));
            float mx = fmaxf(lm[0], lm[1]);
#pragma unroll
            for (int r = 0; r < 4; ++r) mx = fmaxf(mx, fmaxf(la[r], lb[r]));
            mx = xr16(mx, true);
            const float q0 = ex2(lm[0] - mx), q1 = ex2(lm[1] - mx);
            float sum = q0 + q1;
#pragma unroll
            for (int r = 0; r < 4; ++r) { la[r] = ex2(la[r] - mx); lb[r] = ex2(lb[r] - mx); sum += la[r] + lb[r]; }
            const float rc = __builtin_amdgcn_rcpf(xr16(sum, false));
            stc16(ws, l * 16, O_PX + (s * 16 + h) * 2048u + w * 1024,
                  u4{pk(la[0] * rc, lb[0] * rc), pk(la[1] * rc, lb[1] * rc), pk(la[2] * rc, lb[2] * rc), pk(la[3] * rc, lb[3] * rc)});
            stc4(ws, ((2 * ((h & 3) >> 1) + (g >> 1)) * 16 + c) * 16 + (2 * (g & 1) + (h & 1)) * 4, O_PM + (s * 4 + (h >> 2)) * 2048u + w * 1024,
                 pk(q0 * rc, q1 * rc));
            drain();
        }
        bar();
        if (t == 0) stc4(ws, h * 4, F_K + s * 256, 1u);
    }
}

DEV void role_O(const Args& a, lptr lds, int v, int s0) {
    const int t = threadIdx.x, w = __builtin_amdgcn_readfirstlane(t >> 6), l = t & 63, g = l >> 4, c = l & 15;
    rsrc_t ws = mkrs(a.ws, WS_BYTES), ro = mkrs(a.out, 2048 * 896);
    const int tok = t >> 3, fq = t & 7;
    const u2 b2v = ld8(mkrs(a.b2, 896), fq * 8, v * 64);
    bool alive = true;
    int par = 0;
    for (int s = s0; s < 64; s += 6, par ^= 1) {
        wait_flags(ws, F_U + (s >> 1) * 256, 64, alive);
        wait_flags(ws, F_B + (s >> 2) * 256, 4, alive);
        u4 U[4][2];
#pragma unroll
        for (int i = 0; i < 4; ++i)
#pragma unroll
            for (int tt = 0; tt < 2; ++tt) U[i][tt] = ldc16(ws, l * 16, O_UB + ((s * 14 + v) * 16 + 4 * w + i) * 2048u + tt * 1024);
        f4 bb = {bflo(b2v.x), bfhi(b2v.x), bflo(b2v.y), bfhi(b2v.y)};
#pragma unroll
        for (int G = 0; G < 4; ++G) bb += __builtin_bit_cast(f4, ldc16(ws, tok * 1792 + fq * 16, O_BM + G * BMSZ + s * 32 * 1792u + v * 128));
        const float gt = __builtin_bit_cast(float, ldc4(ws, tok * 4, O_GT + s * 128));
        bb *= 1.0f - gt;
        wait_flags(ws, F_P + min(s + 1, 63) * 256, 1, alive);
        if (s < 62) wait_flags(ws, F_K + s * 256, 16, alive);
        u4 W0, W1, pmv[2], P[2][4];
        for (unsigned spins = 0;;) { asm volatile("" ::: "memory");
            W0 = ldc16(ws, l * 16, O_WB + ((s * 14 + v) * 4 + w) * 2048u); W1 = ldc16(ws, l * 16 + 1024, O_WB + ((s * 14 + v) * 4 + w) * 2048u);
            unsigned m = umax4(W0, umax4(W1, 0));
#pragma unroll
            for (int e = 0; e < 2; ++e) {
                pmv[e] = ldc16(ws, l * 16, O_PM + (s * 4 + w) * 2048u + e * 1024);
#pragma unroll
                for (int i = 0; i < 4; ++i) P[e][i] = ldc16(ws, l * 16, O_PX + (s * 16 + 4 * w + i) * 2048u + e * 1024);
            }
            if (s < 62 || !alive) break;
#pragma unroll
            for (int e = 0; e < 2; ++e) m = umax4(pmv[e], umax4(P[e][0], umax4(P[e][1], umax4(P[e][2], umax4(P[e][3], m)))));
            if (__all(m != ~0u)) break;
            if (spin_fail(ws, spins)) alive = false;
            __builtin_amdgcn_s_sleep(4);
        }
        const f4 Z = {0, 0, 0, 0};
        f4 acc[2][2];
#pragma unroll
        for (int e = 0; e < 2; ++e) {
            acc[e][0] = mfma(pmv[e], W0, Z); acc[e][1] = mfma(pmv[e], W1, Z);
#pragma unroll
            for (int i = 0; i < 4; ++i) { acc[e][0] = mfma(P[e][i], U[i][0], acc[e][0]); acc[e][1] = mfma(P[e][i], U[i][1], acc[e][1]); }
        }
        lptr base = lds + par * 18432;
#pragma unroll
        for (int e = 0; e < 2; ++e)
#pragma unroll
            for (int tt = 0; tt < 2; ++tt)
#pragma unroll
                for (int r = 0; r < 4; ++r) *(LAS float*)(base + w * 4608 + (16 * e + 4 * g + r) * 144 + (16 * tt + c) * 4) = acc[e][tt][r];
        bar();
        lptr rr = base + tok * 144 + fq * 16;
        const f4 o = (lrf4(rr) + lrf4(rr + 4608)) + (lrf4(rr + 9216) + lrf4(rr + 13824));
        __builtin_amdgcn_raw_buffer_store_b64(u2{pk(gt * o[0] + bb[0], gt * o[1] + bb[1]), pk(gt * o[2] + bb[2], gt * o[3] + bb[3])}, ro,
                                              tok * 896 + fq * 8, s * 32 * 896 + v * 64, 0);
    }
}

DEV void wfirst(u4 (&Wf)[4][7], rsrc_t ws, unsigned wb, int l) {
#pragma unroll
    for (int k = 0; k < 3; ++k)
#pragma unroll
        for (int n = 0; n < 7; ++n) Wf[k][n] = ld16(ws, l * 16 + k * 1024, wb + n * 14336);
}
template <int NT, bool SWAP>
DEV void gemm_core(f4 (&acc)[7][NT], u4 (&Wf)[4][7], rsrc_t ws, unsigned wb, lptr xl, int l) {
    u4 X[3];
    X[0] = lr16(xl); X[1] = lr16(xl + 14 * 1024);
#pragma unroll
    for (int i = 0; i < 14 * NT; ++i) {
        const int kk = i / NT, e = i % NT;
        if (i + 2 < 14 * NT) X[(i + 2) % 3] = lr16(xl + (((i + 2) % NT) * 14 + (i + 2) / NT) * 1024);
        if (kk + 3 < 14) {
#pragma unroll
            for (int n = e * 8 / NT; n < (e + 1) * 8 / NT; ++n) if (n < 7) Wf[(kk + 3) & 3][n] = ld16(ws, l * 16, wb + n * 14336 + (kk + 3) * 1024);
        }
        sbar();
#pragma unroll
        for (int n = 0; n < 7; ++n) acc[n][e] = SWAP ? mfma(X[i % 3], Wf[kk & 3][n], acc[n][e]) : mfma(Wf[kk & 3][n], X[i % 3], acc[n][e]);
        sbar();
    }
}
template <int NT>
DEV void tile_in(rsrc_t ws, lptr lds, unsigned src, int w, int l) {
#pragma unroll
    for (int i = 0; i < NT * 7 / 2; ++i) dma16(ws, lds + (w * (NT * 7 / 2) + i) * 1024, l * 16, src + (w * (NT * 7 / 2) + i) * 1024);
}

DEV int tid() { int t = threadIdx.x; asm volatile("" : "+v"(t)); return t; }
DEV void publish(rsrc_t ws, unsigned off, int n = 1) {
    drain(); bar();
    if (threadIdx.x < n) stc4(ws, (threadIdx.x >> 2) * 256 + (threadIdx.x & 3) * 4, off, 1u);
}

template <bool ISU, int NT>
DEV void task_proj(const Args& a, lptr lds, int h, int t0) {
    const int t = tid(), w = __builtin_amdgcn_readfirstlane(t >> 6), l = t & 63, g = l >> 4, c = l & 15;
    rsrc_t ws = mkrs(a.ws, WS_BYTES);
    const unsigned wb = (ISU ? O_CW : O_AW) + (h * 28 + 7 * w) * 14336u;
    tile_in<NT>(ws, lds, O_XA + t0 * 14336u, w, l);
    u4 Wf[4][7];
    wfirst(Wf, ws, wb, l);
    f4 acc[7][NT];
#pragma unroll
    for (int n = 0; n < 7; ++n)
#pragma unroll
        for (int e = 0; e < NT; ++e) acc[n][e] = f4{0, 0, 0, 0};
    asm volatile("s_waitcnt vmcnt(21)\n\ts_barrier" ::: "memory");
    gemm_core<NT, ISU>(acc, Wf, ws, wb, lds + l * 16, l);
    if (ISU) {
#pragma unroll
        for (int sg = 0; sg < NT / 2; ++sg)
#pragma unroll
            for (int n = 0; n < 7; ++n) {
                const int nn = 7 * w + n;
                const f4 x = acc[n][2 * sg], y = acc[n][2 * sg + 1];
                stc16(ws, l * 16, O_UB + (((t0 / 2 + sg) * 14 + (nn >> 1)) * 16 + h) * 2048u + (nn & 1) * 1024,
                      u4{pk(x[0], y[0]), pk(x[1], y[1]), pk(x[2], y[2]), pk(x[3], y[3])});
            }
    } else {
#pragma unroll
        for (int e = 0; e < NT; ++e) {
            const unsigned so = O_QB + ((t0 / 2 + (e >> 1)) * 16 + h) * 28672u + (e & 1) * 14336;
#pragma unroll
            for (int p = 0; p < 3; ++p) {
                const f4 x = acc[2 * p][e], y = acc[2 * p + 1][e];
                stc16(ws, g * 512 + c * 16, so + (p < 2 ? w * 2048 + p * 256 : (4 + (w >> 1)) * 2048 + (w & 1) * 256),
                      u4{pk(x[0], x[1]), pk(y[0], y[1]), pk(x[2], x[3]), pk(y[2], y[3])});
            }
            const f4 z = acc[6][e];
            stc4(ws, g * 512 + c * 16, so + 12288 + (w >> 1) * 256 + (w & 1) * 4, pk(z[0], z[1]));
            stc4(ws, g * 512 + c * 16 + 8, so + 12288 + (w >> 1) * 256 + (w & 1) * 4, pk(z[2], z[3]));
        }
    }
    publish(ws, (ISU ? F_U : F_Q) + (t0 >> 2) * 256 + h * 16, NT);
}

template <bool ISU>
DEV void task_first(const Args& a, lptr lds, int h, int cq) {
    const int t = tid(), w = __builtin_amdgcn_readfirstlane(t >> 6), l = t & 63, g = l >> 4, c = l & 15;
    rsrc_t ws = mkrs(a.ws, WS_BYTES);
    const unsigned wb = (ISU ? O_CW : O_AW) + (h * 28 + 7 * cq + 2 * w) * 14336u, w1 = w < 3 ? 14336 : 0;
    tile_in<4>(ws, lds, O_XA, w, l);
    sbar();
    u4 Wf[14][2];
#pragma unroll
    for (int k = 0; k < 14; ++k) { Wf[k][0] = ld16(ws, l * 16 + k * 1024, wb); Wf[k][1] = ld16(ws, l * 16 + k * 1024, wb + w1); }
    asm volatile("s_waitcnt vmcnt(28)\n\ts_barrier" ::: "memory");
    f4 acc[2][4];
#pragma unroll
    for (int k = 0; k < 14; ++k)
#pragma unroll
        for (int e = 0; e < 4; ++e) {
            const u4 X = lr16(lds + l * 16 + (e * 14 + k) * 1024);
            const f4 Z = {0, 0, 0, 0};
#pragma unroll
            for (int n = 0; n < 2; ++n) acc[n][e] = ISU ? mfma(X, Wf[k][n], k ? acc[n][e] : Z) : mfma(Wf[k][n], X, k ? acc[n][e] : Z);
        }
    if (ISU) {
#pragma unroll
        for (int sg = 0; sg < 2; ++sg)
#pragma unroll
            for (int n = 0; n < 2; ++n) {
                const int nn = 7 * cq + 2 * w + n;
                const f4 x = acc[n][2 * sg], y = acc[n][2 * sg + 1];
                if (n == 0 || w < 3) stc16(ws, l * 16, O_UB + ((sg * 14 + (nn >> 1)) * 16 + h) * 2048u + (nn & 1) * 1024,
                                           u4{pk(x[0], y[0]), pk(x[1], y[1]), pk(x[2], y[2]), pk(x[3], y[3])});
            }
    } else {
#pragma unroll
        for (int e = 0; e < 4; ++e) {
            const unsigned so = O_QB + ((e >> 1) * 16 + h) * 28672u + (e & 1) * 14336;
            const f4 x = acc[0][e], y = acc[1][e];
            if (w < 3) stc16(ws, g * 512 + c * 16, so + (w < 2 ? cq * 2048 + w * 256 : (4 + (cq >> 1)) * 2048 + (cq & 1) * 256),
                             u4{pk(x[0], x[1]), pk(y[0], y[1]), pk(x[2], x[3]), pk(y[2], y[3])});
            else {
                stc4(ws, g * 512 + c * 16, so + 12288 + (cq >> 1) * 256 + (cq & 1) * 4, pk(x[0], x[1]));
                stc4(ws, g * 512 + c * 16 + 8, so + 12288 + (cq >> 1) * 256 + (cq & 1) * 4, pk(x[2], x[3]));
            }
        }
    }
    publish(ws, (ISU ? F_U : F_Q) + h * 16 + cq * 4);
}

DEV float gelu(float u) {
    const float z = u * (0.7978845608028654f + 0.035677408136300125f * u * u);
    return u * __builtin_amdgcn_rcpf(1.0f + ex2(-2.8853900817779268f * z));
}

DEV void task_mlp(const Args& a, lptr lds, int G, int rb) {
    const int t = tid(), w = __builtin_amdgcn_readfirstlane(t >> 6), l = t & 63, g = l >> 4, c = l & 15;
    rsrc_t ws = mkrs(a.ws, WS_BYTES), rb1 = mkrs(a.b1, 3584);
    const unsigned wb = O_W1 + (G * 28 + 7 * w) * 14336u;
    tile_in<8>(ws, lds, O_YA + rb * 8 * 14336u, w, l);
    u4 Wf[4][7];
    wfirst(Wf, ws, wb, l);
    f4 acc[7][8];
#pragma unroll
    for (int n = 0; n < 7; ++n) {
        const u2 bv = ld8(rb1, g * 8, (448 * G + 16 * (7 * w + n)) * 2);
        const f4 bf = {bflo(bv.x), bfhi(bv.x), bflo(bv.y), bfhi(bv.y)};
#pragma unroll
        for (int e = 0; e < 8; ++e) acc[n][e] = bf;
    }
    drain(); bar();
    gemm_core<8, false>(acc, Wf, ws, wb, lds + l * 16, l);
    wfirst(Wf, ws, wb + 4 * SET, l);
    bar();
#pragma unroll
    for (int e = 0; e < 8; ++e) {
#pragma unroll
        for (int p = 0; p < 3; ++p) {
            const f4 x = acc[2 * p][e], y = acc[2 * p + 1][e];
            lw16(lds + (e * 14 + 3 * w + p) * 1024 + l * 16,
                 u4{pk(gelu(x[0]), gelu(x[1])), pk(gelu(x[2]), gelu(x[3])), pk(gelu(y[0]), gelu(y[1])), pk(gelu(y[2]), gelu(y[3]))});
        }
        const f4 z = acc[6][e];
        *(LAS u2*)(lds + (e * 14 + 12 + (w >> 1)) * 1024 + l * 16 + (w & 1) * 8) = u2{pk(gelu(z[0]), gelu(z[1])), pk(gelu(z[2]), gelu(z[3]))};
        sbar();
    }
#pragma unroll
    for (int n = 0; n < 7; ++n)
#pragma unroll
        for (int e = 0; e < 8; ++e) acc[n][e] = f4{0, 0, 0, 0};
    bar();
    gemm_core<8, false>(acc, Wf, ws, wb + 4 * SET, lds + l * 16, l);
#pragma unroll
    for (int e = 0; e < 8; ++e)
#pragma unroll
        for (int n = 0; n < 7; ++n)
            stc16(ws, c * 1792 + g * 16, O_BM + G * BMSZ + (rb * 128 + e * 16) * 1792u + (7 * w + n) * 64, __builtin_bit_cast(u4, acc[n][e]));
    publish(ws, F_B + rb * 256 + G * 4);
}

__global__ void __launch_bounds__(256, 1) main_kernel(Args a) {
    extern __shared__ char lds_raw[];
    lptr lds = (lptr)lds_raw;
    const int b = blockIdx.x, x = b & 7, j = b >> 3;
    if (x == 0) { role_S(a, lds, j >> 1, j & 1); return; }
    asm volatile("" ::: "memory");
    if (x == 1) { if (j < 28) role_V(a, lds, j); return; }
    const int q = (x - 2) * 32 + j;
#pragma unroll 1
    for (int r = 0; r < 4; ++r) {
        int n = 8, h = q / 6, t0 = q - 6 * h;
        if (r == 0) { if (t0 < 4) n = 1; else { n = 4; t0 = 4 * t0 - 12; } }
        else if (r == 1) { if (t0 == 0 || t0 > 3) { n = 4; t0 = t0 ? 4 * t0 - 4 : 20; } else t0 = 16 + 8 * t0; }
        else if (r == 2) t0 = 48 + 8 * t0;
        else if (q < 128) { h = q >> 2; t0 = 96 + 8 * (q & 3); }
        else n = 0;
        const bool isu = h < 16;
        h &= 15;
        if (n == 0) task_mlp(a, lds, (q - 128) >> 4, q & 15);
        else if (n == 1) { if (isu) task_first<true>(a, lds, h, t0); else task_first<false>(a, lds, h, t0); }
        else if (n == 4) { if (isu) task_proj<true, 4>(a, lds, h, t0); else task_proj<false, 4>(a, lds, h, t0); }
        else if (isu) task_proj<true, 8>(a, lds, h, t0); else task_proj<false, 8>(a, lds, h, t0);
    }
    if (j < 16) role_K(a, lds, j, x - 2);
    else if (j < 30) { role_O(a, lds, j - 16, x - 2); }
}

extern "C" void kernel_launch(void* const* d_in, const int* in_sizes, int n_in, void* d_out, int out_size, void* d_ws, size_t ws_size,
                              hipStream_t stream) {
    Args a;
    a.x = d_in[0]; a.mem0 = d_in[1]; a.wq = d_in[2]; a.wk = d_in[3]; a.wv = d_in[4]; a.wo = d_in[5];
    a.w1 = d_in[8]; a.b1 = d_in[9]; a.w2 = d_in[10]; a.b2 = d_in[11]; a.gw = d_in[12]; a.out = d_out; a.ws = d_ws;
    (void)hipFuncSetAttribute((const void*)main_kernel, hipFuncAttributeMaxDynamicSharedMemorySize, LDS_MAIN);
    (void)hipFuncSetAttribute((const void*)prep_kernel, hipFuncAttributeMaxDynamicSharedMemorySize, LDS_PREP);
    prep_kernel<<<K0_BLOCKS, 512, LDS_PREP, stream>>>(a);
    main_kernel<<<256, 256, LDS_MAIN, stream>>>(a);
}
```
